```python
import torch

def _softmax(x, axis=-1):
    return torch.softmax(x, dim=axis)
def _gelu(x):
    return torch.nn.functional.gelu(x, approximate="tanh")
def _silu(x):
    return torch.nn.functional.silu(x)
def _sigmoid(x):
    return torch.sigmoid(x)
def _layernorm(x, axis=-1, eps=1e-5):
    m = x.mean(dim=axis, keepdim=True)
    v = x.var(dim=axis, keepdim=True, unbiased=False)
    return (x - m) / torch.sqrt(v + eps)
def _scatter_add(tgt, idx, src, axis=0):
    return tgt.clone().index_add_(axis, idx, src)
def _segment_sum(src, seg, n):
    out = torch.zeros((n,) + src.shape[1:], dtype=src.dtype)
    return out.index_add_(0, seg, src)
def _pad(x, pw):
    flat = tuple(v for pair in reversed(pw) for v in pair)
    return torch.nn.functional.pad(x, flat)

kernel_name = 'standalone__gqa_block_cached__near_ridge'

def setup_inputs():

    _w = (lambda a: (lambda t: t.to(torch.bfloat16) if t.is_floating_point() else t)(torch.as_tensor(a)))
    B, Sq, D = 16, 32, 640
    Hq, Hkv, Sk, Dh = 32, 32, 32, 192
    assert Hq % Hkv == 0, (Hq, Hkv)
    return {
        "x": _w((lambda *s: torch.randn(*s, dtype=torch.bfloat16) * 0.8)(B, Sq, D)),
        "k_cache": _w((lambda *s: torch.randn(*s, dtype=torch.bfloat16) * 0.8)(B, Hkv, Sk, Dh)),
        "v_cache": _w((lambda *s: torch.randn(*s, dtype=torch.bfloat16) * 0.05)(B, Hkv, Sk, Dh)),
        "wq": _w((lambda *s: torch.randn(*s, dtype=torch.bfloat16) * 0.8)(D, Hq * Dh) * (D ** -0.5)),
        "wo": _w((lambda *s: torch.randn(*s, dtype=torch.bfloat16) * 0.05)(Hq * Dh, D) * ((Hq * Dh) ** -0.5)),
    }

def reference(x, k_cache, v_cache, wq, wo):
    B, Sq, D = x.shape
    Hq, Dh = 32, 192
    Hkv = k_cache.shape[1]
    rep = Hq // Hkv
    q = (lambda x, *d: x.permute(*d))((x @ wq).reshape(B, Sq, Hq, Dh), 0, 2, 1, 3)
    k = (lambda x, n, axis: torch.repeat_interleave(x, n, dim=axis))(k_cache, rep, axis=1)
    v = (lambda x, n, axis: torch.repeat_interleave(x, n, dim=axis))(v_cache, rep, axis=1)
    scores = torch.einsum("bhqd,bhkd->bhqk", q, k) * (Dh ** -0.5)
    p = _softmax(scores, axis=-1)
    o = torch.einsum("bhqk,bhkd->bhqd", p, v)
    return (lambda x, *d: x.permute(*d))(o, 0, 2, 1, 3).reshape(B, Sq, Hq * Dh) @ wo
```

```cpp
#include <hip/hip_runtime.h>
#include <hip/hip_bf16.h>
#include <stdint.h>

#define LAS __attribute__((address_space(3)))
#define DI __device__ __forceinline__
typedef short s16x4 __attribute__((ext_vector_type(4)));
typedef short s16x8 __attribute__((ext_vector_type(8)));
typedef __bf16 bf16x8 __attribute__((ext_vector_type(8)));
typedef __bf16 bf16x2 __attribute__((ext_vector_type(2)));
typedef float f32x2 __attribute__((ext_vector_type(2)));
typedef float f32x4 __attribute__((ext_vector_type(4)));
typedef float f32x16 __attribute__((ext_vector_type(16)));
typedef unsigned u32x2 __attribute__((ext_vector_type(2)));
typedef unsigned u32x4 __attribute__((ext_vector_type(4)));

constexpr int NB = 16, SQ = 32, DM = 640, NH = 32, SK = 32, DH = 192, HD = NH * DH;
constexpr int SLOT = 32768, NSLOT = 4;
constexpr int MISC = SLOT * NSLOT;
constexpr int LDS_BYTES = MISC + 8192;
constexpr size_t WS_P = 0;
constexpr size_t WS_P_BYTES = (size_t)8 * 32 * 2 * 2048;
constexpr size_t WS_PART = WS_P + WS_P_BYTES;
constexpr size_t WS_PART_BYTES = (size_t)32 * 8 * 8 * 5120;

struct Args {
    const void* x; const void* kc; const void* vc; const void* wq; const void* wo;
    void* out; char* ws;
};

__shared__ __attribute__((aligned(16))) char smem[LDS_BYTES];

DI bf16x8 as_bf(s16x8 v) { return __builtin_bit_cast(bf16x8, v); }
DI unsigned pk2(float a, float b) {
    f32x2 v = {a, b};
    bf16x2 r = __builtin_convertvector(v, bf16x2);
    return __builtin_bit_cast(unsigned, r);
}
DI s16x8 lds_b128(unsigned addr) { return *(LAS s16x8*)(uintptr_t)addr; }
DI s16x4 lds_tr(unsigned addr) { return __builtin_amdgcn_ds_read_tr16_b64_v4i16((LAS s16x4*)(uintptr_t)addr); }
DI s16x8 cat(s16x4 a, s16x4 b) { return __builtin_shufflevector(a, b, 0, 1, 2, 3, 4, 5, 6, 7); }
#define MFMA32(a, b, c) __builtin_amdgcn_mfma_f32_32x32x16_bf16(as_bf(a), as_bf(b), (c), 0, 0, 0)
#define MFMA16(a, b, c) __builtin_amdgcn_mfma_f32_16x16x32_bf16(as_bf(a), as_bf(b), (c), 0, 0, 0)
#define BARRIER_LGKM() asm volatile("s_waitcnt lgkmcnt(0)\n\ts_barrier" ::: "memory")

constexpr int n_dma(int t) { return t < 0 ? 0 : t < 10 ? 8 : t == 10 ? 6 : t < 23 ? 7 : 0; }

template <int N> DI void wait_vm_barrier() {
    asm volatile("s_waitcnt vmcnt(%0)\n\ts_barrier" ::"n"(N) : "memory");
}

struct Loader {
    __amdgpu_buffer_rsrc_t rx, rk, rv, rwq, rwo;
    unsigned vX[2], vW[6], vK[6], vV[2], vO[5];
    unsigned lw;
    unsigned xg;
};

template <int T> DI void issue_stage(const Loader& L) {
    LAS char* base = (LAS char*)smem + (T % NSLOT) * SLOT;
    if constexpr (T < 10) {
#pragma unroll
        for (int e = 0; e < 2; ++e)
            __builtin_amdgcn_raw_ptr_buffer_load_lds(L.rx, (LAS void*)(base + (L.lw * 2 + e) * 1024), 16, L.vX[e], T * 128, 0, 0);
#pragma unroll
        for (int e = 0; e < 6; ++e)
            __builtin_amdgcn_raw_ptr_buffer_load_lds(L.rwq, (LAS void*)(base + 8192 + (L.lw * 6 + e) * 1024), 16, L.vW[e], T * 64 * (HD * 2), 0, 0);
    } else if constexpr (T == 10) {
#pragma unroll
        for (int e = 0; e < 6; ++e)
            __builtin_amdgcn_raw_ptr_buffer_load_lds(L.rk, (LAS void*)(base + (L.lw * 6 + e) * 1024), 16, L.vK[e], 0, 0, 0);
    } else if constexpr (T < 23) {
        constexpr int u = T - 11, hh = u / 3, dc = u % 3;
        const unsigned sv = (L.xg * 4 + hh) * (SK * DH * 2) + dc * 128;
        const unsigned so = ((L.xg * 4 + hh) * DH + dc * 64) * (DM * 2);
#pragma unroll
        for (int e = 0; e < 2; ++e)
            __builtin_amdgcn_raw_ptr_buffer_load_lds(L.rv, (LAS void*)(base + (L.lw * 2 + e) * 1024), 16, L.vV[e], sv, 0, 0);
#pragma unroll
        for (int e = 0; e < 5; ++e)
            __builtin_amdgcn_raw_ptr_buffer_load_lds(L.rwo, (LAS void*)(base + 8192 + (L.lw * 5 + e) * 1024), 16, L.vO[e], so, 0, 0);
    }
}

template <int T, int T0, int T1> DI void loader_steps(const Loader& L) {
    if constexpr (T < T1) {
        constexpr int pend = (T + 1 < T1 ? n_dma(T + 1) : 0) + (T + 2 < T1 ? n_dma(T + 2) : 0);
        wait_vm_barrier<pend>();
        if constexpr (T + 3 < T1) issue_stage<T + 3>(L);
        loader_steps<T + 1, T0, T1>(L);
    }
}

template <int MODE>
__global__ void __launch_bounds__(512, 2) gqa_main(Args a) {
    constexpr int T0 = MODE == 2 ? 11 : 0, T1 = MODE == 1 ? 11 : 23;
    const int tid = threadIdx.x;
    const int lane = tid & 63;
    const int wave = __builtin_amdgcn_readfirstlane(tid >> 6);
    const int xg = blockIdx.x & 7, loc = blockIdx.x >> 3;
    const int j = loc >> 2, i = loc & 3;
    const int head = xg * 4 + i;

    if (wave >= 4) {
        Loader L;
        L.lw = wave - 4; L.xg = xg;
        L.rx = __builtin_amdgcn_make_buffer_rsrc((void*)a.x, 0, NB * SQ * DM * 2, 0x00020000);
        L.rk = __builtin_amdgcn_make_buffer_rsrc((void*)a.kc, 0, NB * NH * SK * DH * 2, 0x00020000);
        L.rv = __builtin_amdgcn_make_buffer_rsrc((void*)a.vc, 0, NB * NH * SK * DH * 2, 0x00020000);
        L.rwq = __builtin_amdgcn_make_buffer_rsrc((void*)a.wq, 0, DM * HD * 2, 0x00020000);
        L.rwo = __builtin_amdgcn_make_buffer_rsrc((void*)a.wo, 0, HD * DM * 2, 0x00020000);
#pragma unroll
        for (int e = 0; e < 2; ++e) {
            const unsigned p = (L.lw * 2 + e) * 64 + lane, row = p >> 3, c = (p & 7) ^ ((row >> 1) & 7);
            L.vX[e] = (64 * j + row) * (DM * 2) + c * 16;
            L.vV[e] = ((2 * j + (row >> 5)) * NH * SK + (row & 31)) * (DH * 2) + c * 16;
        }
#pragma unroll
        for (int e = 0; e < 6; ++e) {
            const unsigned p = (L.lw * 6 + e) * 64 + lane, row = p / 24, cc = p % 24;
            L.vW[e] = row * (HD * 2) + head * (DH * 2) + (cc ^ (((row >> 1) & 1) << 2)) * 16;
            L.vK[e] = (((2 * j + (row >> 5)) * NH + head) * SK + (row & 31)) * (DH * 2) + (cc ^ ((row >> 1) & 7)) * 16;
        }
#pragma unroll
        for (int e = 0; e < 5; ++e) {
            const unsigned p = (L.lw * 5 + e) * 64 + lane, row = p / 20, cc = p % 20;
            L.vO[e] = row * (DM * 2) + i * 320 + (cc ^ (((row >> 3) & 1) << 1)) * 16;
        }
        issue_stage<T0>(L); issue_stage<T0 + 1>(L); issue_stage<T0 + 2>(L);
        loader_steps<T0, T0, T1>(L);
        if constexpr (MODE == 1) asm volatile("s_barrier" ::: "memory");
        return;
    }

    const int wb = wave >> 1, wd = wave & 1;
    if constexpr (MODE != 2) {
        const int h = lane >> 5, r = lane & 31;
        unsigned aW[3];
        {
            const int grp = (lane >> 4) & 1, q = (lane >> 2) & 3, p = lane & 3;
            const int bs = ((p & 1) << 1) | (p >> 1);
            const int m = (q >> 1) & 1;
#pragma unroll
            for (int t = 0; t < 3; ++t)
                aW[t] = 8192 + (8 * h + q) * 384 + grp * 32 + bs * 8 + (((3 * wd + t) ^ m) * 64);
        }
        unsigned aX[4];
        {
            const int row = 32 * wb + r;
#pragma unroll
            for (int ks = 0; ks < 4; ++ks) aX[ks] = (row * 128 + ((h ^ ((row >> 1) & 7)) * 16)) ^ (32 * ks);
        }
        f32x16 acc[3];
#pragma unroll
        for (int t = 0; t < 3; ++t)
#pragma unroll
            for (int e = 0; e < 16; ++e) acc[t][e] = 0.f;
        s16x8 A0[2][3], B0[2], A1[2][3], B1[2];
        auto rd = [&](s16x8 (&A)[2][3], s16x8 (&Bf)[2], unsigned so, int ds) {
#pragma unroll
            for (int s = 0; s < 2; ++s) {
                const int ks = 2 * ds + s;
                Bf[s] = lds_b128(aX[ks] + so);
#pragma unroll
                for (int t = 0; t < 3; ++t)
                    A[s][t] = cat(lds_tr(aW[t] + so + ks * 16 * 384), lds_tr(aW[t] + so + (ks * 16 + 4) * 384));
            }
        };
        auto mm = [&](s16x8 (&A)[2][3], s16x8 (&Bf)[2]) {
#pragma unroll
            for (int s = 0; s < 2; ++s)
#pragma unroll
                for (int t = 0; t < 3; ++t) acc[t] = MFMA32(A[s][t], Bf[s], acc[t]);
        };
        BARRIER_LGKM();
        rd(A0, B0, 0, 0);
        rd(A1, B1, 0, 1);
        mm(A0, B0);
#pragma unroll 1
        for (int t = 1; t < 10; ++t) {
            const unsigned so = (t & 3) * SLOT;
            BARRIER_LGKM();
            rd(A0, B0, so, 0);
            mm(A1, B1);
            rd(A1, B1, so, 1);
            mm(A0, B0);
        }
        BARRIER_LGKM();
        s16x8 Kf[3][2];
        {
            const int row = 32 * wb + r;
            const unsigned kb = (10 % NSLOT) * SLOT + row * 384;
            const int m = (row >> 1) & 7;
#pragma unroll
            for (int t = 0; t < 3; ++t)
#pragma unroll
                for (int s = 0; s < 2; ++s) {
                    const int c = 12 * wd + 4 * t + 2 * s + h;
                    Kf[t][s] = lds_b128(kb + ((c & ~7) | ((c & 7) ^ m)) * 16);
                }
        }
        mm(A1, B1);
        f32x16 S;
#pragma unroll
        for (int e = 0; e < 16; ++e) S[e] = 0.f;
#pragma unroll
        for (int t = 0; t < 3; ++t)
#pragma unroll
            for (int s = 0; s < 2; ++s) {
                u32x4 qp;
#pragma unroll
                for (int e = 0; e < 4; ++e) qp[e] = pk2(acc[t][8 * s + 2 * e], acc[t][8 * s + 2 * e + 1]);
                S = MFMA32(Kf[t][s], __builtin_bit_cast(s16x8, qp), S);
            }
        const unsigned sx = MISC + wb * 4096 + lane * 16;
        if (wd == 1) {
#pragma unroll
            for (int gq = 0; gq < 4; ++gq) {
                f32x4 v = {S[4 * gq], S[4 * gq + 1], S[4 * gq + 2], S[4 * gq + 3]};
                *(LAS f32x4*)(uintptr_t)(sx + gq * 1024) = v;
            }
        }
        BARRIER_LGKM();
        if (wd == 0) {
#pragma unroll
            for (int gq = 0; gq < 4; ++gq) {
                const f32x4 v = *(LAS f32x4*)(uintptr_t)(sx + gq * 1024);
#pragma unroll
                for (int e = 0; e < 4; ++e) S[4 * gq + e] += v[e];
            }
            const float c = 0.07216878364870322f * 1.4426950408889634f;
            float mx = S[0];
#pragma unroll
            for (int e = 1; e < 16; ++e) mx = fmaxf(mx, S[e]);
            mx = fmaxf(mx, __shfl_xor(mx, 32));
            float sum = 0.f;
#pragma unroll
            for (int e = 0; e < 16; ++e) { S[e] = __builtin_amdgcn_exp2f((S[e] - mx) * c); sum += S[e]; }
            sum += __shfl_xor(sum, 32);
            const float inv = 1.0f / sum;
            u32x4 f0, f1;
#pragma unroll
            for (int e = 0; e < 2; ++e) {
                f0[e] = pk2(S[2 * e] * inv, S[2 * e + 1] * inv);
                f0[2 + e] = pk2(S[8 + 2 * e] * inv, S[8 + 2 * e + 1] * inv);
                f1[e] = pk2(S[4 + 2 * e] * inv, S[4 + 2 * e + 1] * inv);
                f1[2 + e] = pk2(S[12 + 2 * e] * inv, S[12 + 2 * e + 1] * inv);
            }
            char* pb = a.ws + WS_P + ((size_t)(j * NH + head) * 2 + wb) * 2048 + (r >> 4) * 1024 + (r & 15) * 16;
            *(u32x4*)(pb + h * 256) = f0;
            *(u32x4*)(pb + (2 + h) * 256) = f1;
        }
    }
    if constexpr (MODE != 1) {
        const int g = lane >> 4, c = lane & 15;
        unsigned aV[2], aO[2];
        {
            const int row = 32 * wb + c;
            aV[0] = row * 128 + ((g ^ ((c >> 1) & 7)) * 16);
            aV[1] = aV[0] ^ 64;
            const int q = (lane >> 2) & 3, p = lane & 3, m = g & 1;
            const unsigned com = 8192 + (8 * g + q) * 320 + p * 8;
            aO[0] = com + ((5 * wd) ^ m) * 32;
            aO[1] = com + ((5 * wd + 1) ^ m) * 32;
        }
        f32x4 acc[2][5];
        u32x4 vw[4][5];
        s16x8 A0[2], B0[5], A1[2], B1[5];
        auto rd = [&](s16x8 (&A)[2], s16x8 (&Bf)[5], unsigned so, int ks) {
#pragma unroll
            for (int kt = 0; kt < 2; ++kt) A[kt] = lds_b128(aV[ks] + so + kt * 2048);
#pragma unroll
            for (int nt = 0; nt < 5; ++nt) {
                const unsigned ad = aO[nt & 1] + so + (nt & ~1) * 32 + ks * 32 * 320;
                Bf[nt] = cat(lds_tr(ad), lds_tr(ad + 4 * 320));
            }
        };
        auto mm = [&](s16x8 (&A)[2], s16x8 (&Bf)[5]) {
#pragma unroll
            for (int nt = 0; nt < 5; ++nt)
#pragma unroll
                for (int kt = 0; kt < 2; ++kt) acc[kt][nt] = MFMA16(A[kt], Bf[nt], acc[kt][nt]);
        };
        auto zero = [&]() {
#pragma unroll
            for (int kt = 0; kt < 2; ++kt)
#pragma unroll
                for (int nt = 0; nt < 5; ++nt) acc[kt][nt] = (f32x4){0.f, 0.f, 0.f, 0.f};
        };
        auto pack = [&](u32x4 (&o)[5]) {
#pragma unroll
            for (int nt = 0; nt < 5; ++nt) {
                o[nt][0] = pk2(acc[0][nt][0], acc[0][nt][1]); o[nt][1] = pk2(acc[0][nt][2], acc[0][nt][3]);
                o[nt][2] = pk2(acc[1][nt][0], acc[1][nt][1]); o[nt][3] = pk2(acc[1][nt][2], acc[1][nt][3]);
            }
        };
#pragma unroll
        for (int hh = 0; hh < 4; ++hh) {
#pragma unroll
            for (int dc = 0; dc < 3; ++dc) {
                const int t = 11 + 3 * hh + dc;
                const unsigned so = (t % NSLOT) * SLOT;
                if (MODE == 2 || t > 11) BARRIER_LGKM();
                rd(A0, B0, so, 0);
                if (hh + dc > 0) mm(A1, B1);
                if (dc == 0) { if (hh > 0) pack(vw[hh - 1]); zero(); }
                rd(A1, B1, so, 1);
                mm(A0, B0);
            }
        }
        mm(A1, B1);
        pack(vw[3]);
        f32x4 o[5][2];
#pragma unroll
        for (int nt = 0; nt < 5; ++nt) { o[nt][0] = (f32x4){0.f, 0.f, 0.f, 0.f}; o[nt][1] = o[nt][0]; }
#pragma unroll
        for (int hh = 0; hh < 4; ++hh) {
            const char* pb = a.ws + WS_P + ((size_t)(j * NH + xg * 4 + hh) * 2 + wb) * 2048 + lane * 16;
            const s16x8 p0 = *(const s16x8*)pb, p1 = *(const s16x8*)(pb + 1024);
#pragma unroll
            for (int nt = 0; nt < 5; ++nt) {
                o[nt][0] = MFMA16(__builtin_bit_cast(s16x8, vw[hh][nt]), p0, o[nt][0]);
                o[nt][1] = MFMA16(__builtin_bit_cast(s16x8, vw[hh][nt]), p1, o[nt][1]);
            }
        }
        char* pp = a.ws + WS_PART + ((size_t)((j * 4 + i) * 8 + xg) * 8) * 5120 + (wb * 4 + (c >> 3)) * 5120 + wd * 2560 + g * 128 + (c & 7) * 16;
#pragma unroll
        for (int nt = 0; nt < 5; ++nt)
#pragma unroll
            for (int qt = 0; qt < 2; ++qt) *(f32x4*)(pp + qt * 2 * 5120 + nt * 512) = o[nt][qt];
    }
}

__global__ void __launch_bounds__(320) gqa_reduce(Args a) {
    const int xg = blockIdx.x & 7, loc = blockIdx.x >> 3;
    const int j = loc >> 2, i = loc & 3;
    const int L = threadIdx.x;
    const char* pp = a.ws + WS_PART + ((size_t)(loc * 8) * 8 + xg) * 5120 + L * 16;
    f32x4 v[8];
#pragma unroll
    for (int s = 0; s < 8; ++s) v[s] = *(const f32x4*)(pp + (size_t)s * 8 * 5120);
    f32x4 t = ((v[0] + v[1]) + (v[2] + v[3])) + ((v[4] + v[5]) + (v[6] + v[7]));
    const int cl = L & 7, g = (L >> 3) & 3, nn = L >> 5;
    const int row = 64 * j + 8 * xg + cl, n = 160 * i + 16 * nn + 4 * g;
    u32x2 o = {pk2(t[0], t[1]), pk2(t[2], t[3])};
    *(u32x2*)((char*)a.out + ((size_t)row * DM + n) * 2) = o;
}

extern "C" void kernel_launch(void* const* d_in, const int* in_sizes, int n_in, void* d_out, int out_size,
                              void* d_ws, size_t ws_size, hipStream_t stream) {
    Args a;
    a.x = d_in[0]; a.kc = d_in[1]; a.vc = d_in[2]; a.wq = d_in[3]; a.wo = d_in[4];
    a.out = d_out; a.ws = (char*)d_ws;
    gqa_main<1><<<256, 512, 0, stream>>>(a);
    gqa_main<2><<<256, 512, 0, stream>>>(a);
    gqa_reduce<<<256, 320, 0, stream>>>(a);
}
```

```cpp
#include <hip/hip_runtime.h>
#include <hip/hip_bf16.h>
#include <stdint.h>

#define LAS __attribute__((address_space(3)))
#define GAS __attribute__((address_space(1)))
#define DI __device__ __forceinline__
typedef short s16x4 __attribute__((ext_vector_type(4)));
typedef short s16x8 __attribute__((ext_vector_type(8)));
typedef __bf16 bf16x8 __attribute__((ext_vector_type(8)));
typedef __bf16 bf16x2 __attribute__((ext_vector_type(2)));
typedef float f32x2 __attribute__((ext_vector_type(2)));
typedef float f32x4 __attribute__((ext_vector_type(4)));
typedef float f32x16 __attribute__((ext_vector_type(16)));
typedef unsigned u32x2 __attribute__((ext_vector_type(2)));
typedef unsigned u32x4 __attribute__((ext_vector_type(4)));
typedef unsigned long long u64;
typedef GAS u64 gu64;

constexpr int NB = 16, SQ = 32, DM = 640, NH = 32, SK = 32, DH = 192, HD = NH * DH;
constexpr int SLOT = 32768, NSLOT = 4;
constexpr int MISC = SLOT * NSLOT;
constexpr int TAGW = MISC + 8192;
constexpr int LDS_BYTES = TAGW + 64;
constexpr unsigned WS_EPOCH = 0;
constexpr unsigned WS_F1 = 4096;
constexpr unsigned WS_F2 = WS_F1 + 256 * 128;
constexpr unsigned WS_P = 1 << 17;
constexpr unsigned WS_P_BYTES = 8 * 32 * 2 * 2048;
constexpr unsigned WS_PART = WS_P + WS_P_BYTES;
constexpr unsigned WS_PART_BYTES = 32 * 8 * 8 * 5120;
constexpr unsigned WS_USED = WS_PART + WS_PART_BYTES;
constexpr unsigned kSpinCap = 1u << 18;

struct Args {
    const void* x; const void* kc; const void* vc; const void* wq; const void* wo;
    void* out; char* ws;
};

__shared__ __attribute__((aligned(16))) char smem[LDS_BYTES];

DI bf16x8 as_bf(s16x8 v) { return __builtin_bit_cast(bf16x8, v); }
DI unsigned pk2(float a, float b) {
    f32x2 v = {a, b};
    bf16x2 r = __builtin_convertvector(v, bf16x2);
    return __builtin_bit_cast(unsigned, r);
}
DI s16x8 lds_b128(unsigned addr) { return *(LAS s16x8*)(uintptr_t)addr; }
DI s16x4 lds_tr(unsigned addr) { return __builtin_amdgcn_ds_read_tr16_b64_v4i16((LAS s16x4*)(uintptr_t)addr); }
DI s16x8 cat(s16x4 a, s16x4 b) { return __builtin_shufflevector(a, b, 0, 1, 2, 3, 4, 5, 6, 7); }
#define MFMA32(a, b, c) __builtin_amdgcn_mfma_f32_32x32x16_bf16(as_bf(a), as_bf(b), (c), 0, 0, 0)
#define MFMA16(a, b, c) __builtin_amdgcn_mfma_f32_16x16x32_bf16(as_bf(a), as_bf(b), (c), 0, 0, 0)
#define BARRIER_LGKM() asm volatile("s_waitcnt lgkmcnt(0)\n\ts_barrier" ::: "memory")
#define BARRIER_VM0() asm volatile("s_waitcnt vmcnt(0) lgkmcnt(0)\n\ts_barrier" ::: "memory")
#define SCHED0() __builtin_amdgcn_sched_barrier(0)
#define SGB(m, n) __builtin_amdgcn_sched_group_barrier(m, n, 0)
#define SGB_P1() do { SGB(0x8, 1); SGB(0x100, 5); SGB(0x8, 1); SGB(0x100, 5); SGB(0x8, 1); SGB(0x100, 4); SGB(0x8, 3); } while (0)
#define SGB_P2() do { SGB(0x8, 1); SGB(0x100, 2); SGB(0x8, 1); SGB(0x100, 2); SGB(0x8, 1); SGB(0x100, 2); SGB(0x8, 1); SGB(0x100, 2); \
                      SGB(0x8, 1); SGB(0x100, 2); SGB(0x8, 1); SGB(0x100, 2); SGB(0x8, 4); } while (0)
#define RLX_AGENT __ATOMIC_RELAXED, __HIP_MEMORY_SCOPE_AGENT

#define STAMP_DECL
#define ST(id)
#define STS(id)
#define STR(id)
#define ST_FLUSH(role)

DI void wait_tags(const gu64* word, u64 tag, bool active, u64 first) {
    u64 v = first;
    for (unsigned spins = 0; !__all(!active || v == tag); ++spins) {
        if (spins > kSpinCap) break;
        __builtin_amdgcn_s_sleep(1);
        if (active) v = __hip_atomic_load(word, RLX_AGENT);
    }
}

constexpr int T1 = 23;
constexpr int n_dma(int t) { return t < 0 ? 0 : t < 10 ? 8 : t == 10 ? 6 : t < T1 ? 7 : 0; }

template <int N> DI void wait_vm_barrier() {
    asm volatile("s_waitcnt vmcnt(%0)\n\ts_barrier" ::"n"(N) : "memory");
}

struct Loader {
    __amdgpu_buffer_rsrc_t rx, rk, rv, rwq, rwo;
    unsigned vX[2], vW[6], vK[6], vV[2], vO[5];
    unsigned lw;
    unsigned xg;
};

template <int T> DI void issue_stage(const Loader& L) {
    LAS char* base = (LAS char*)smem + (T % NSLOT) * SLOT;
    if constexpr (T < 10) {
#pragma unroll
        for (int e = 0; e < 2; ++e)
            __builtin_amdgcn_raw_ptr_buffer_load_lds(L.rx, (LAS void*)(base + (L.lw * 2 + e) * 1024), 16, L.vX[e], T * 128, 0, 0);
#pragma unroll
        for (int e = 0; e < 6; ++e)
            __builtin_amdgcn_raw_ptr_buffer_load_lds(L.rwq, (LAS void*)(base + 8192 + (L.lw * 6 + e) * 1024), 16, L.vW[e], T * 64 * (HD * 2), 0, 0);
    } else if constexpr (T == 10) {
#pragma unroll
        for (int e = 0; e < 6; ++e)
            __builtin_amdgcn_raw_ptr_buffer_load_lds(L.rk, (LAS void*)(base + (L.lw * 6 + e) * 1024), 16, L.vK[e], 0, 0, 0);
    } else if constexpr (T < T1) {
        constexpr int u = T - 11, hh = u / 3, dc = u % 3;
        const unsigned sv = (L.xg * 4 + hh) * (SK * DH * 2) + dc * 128;
        const unsigned so = ((L.xg * 4 + hh) * DH + dc * 64) * (DM * 2);
#pragma unroll
        for (int e = 0; e < 2; ++e)
            __builtin_amdgcn_raw_ptr_buffer_load_lds(L.rv, (LAS void*)(base + (L.lw * 2 + e) * 1024), 16, L.vV[e], sv, 0, 0);
#pragma unroll
        for (int e = 0; e < 5; ++e)
            __builtin_amdgcn_raw_ptr_buffer_load_lds(L.rwo, (LAS void*)(base + 8192 + (L.lw * 5 + e) * 1024), 16, L.vO[e], so, 0, 0);
    }
}

template <int T> DI void loader_steps(const Loader& L, unsigned& stv, int lane) {
    if constexpr (T < T1) {
        constexpr int pend = n_dma(T + 1) + n_dma(T + 2);
        wait_vm_barrier<pend>();
        STS(2 + T);
        if constexpr (T + 3 < T1) issue_stage<T + 3>(L);
        STS(30 + T);
        loader_steps<T + 1>(L, stv, lane);
    }
}

__global__ void __launch_bounds__(512, 2) gqa_fused(Args a) {
    const int tid = threadIdx.x;
    const int lane = tid & 63;
    const int wave = __builtin_amdgcn_readfirstlane(tid >> 6);
    const int xg = blockIdx.x & 7, loc = blockIdx.x >> 3;
    const int j = loc >> 2, i = loc & 3;
    const int head = xg * 4 + i;
    const __amdgpu_buffer_rsrc_t rws = __builtin_amdgcn_make_buffer_rsrc((void*)a.ws, 0, WS_USED, 0x00020000);
    STAMP_DECL; STR(62); ST(0);
    unsigned stv = 0;

    if (wave >= 4) {
        Loader L;
        L.lw = wave - 4; L.xg = xg;
        L.rx = __builtin_amdgcn_make_buffer_rsrc((void*)a.x, 0, NB * SQ * DM * 2, 0x00020000);
        L.rk = __builtin_amdgcn_make_buffer_rsrc((void*)a.kc, 0, NB * NH * SK * DH * 2, 0x00020000);
        L.rv = __builtin_amdgcn_make_buffer_rsrc((void*)a.vc, 0, NB * NH * SK * DH * 2, 0x00020000);
        L.rwq = __builtin_amdgcn_make_buffer_rsrc((void*)a.wq, 0, DM * HD * 2, 0x00020000);
        L.rwo = __builtin_amdgcn_make_buffer_rsrc((void*)a.wo, 0, HD * DM * 2, 0x00020000);
#pragma unroll
        for (int e = 0; e < 2; ++e) {
            const unsigned p = (L.lw * 2 + e) * 64 + lane, row = p >> 3, c = (p & 7) ^ ((row >> 1) & 7);
            L.vX[e] = (64 * j + row) * (DM * 2) + c * 16;
            L.vV[e] = ((2 * j + (row >> 5)) * NH * SK + (row & 31)) * (DH * 2) + c * 16;
        }
#pragma unroll
        for (int e = 0; e < 6; ++e) {
            const unsigned p = (L.lw * 6 + e) * 64 + lane, row = p / 24, cc = p % 24;
            L.vW[e] = row * (HD * 2) + head * (DH * 2) + (cc ^ (((row >> 1) & 1) << 2)) * 16;
            L.vK[e] = (((2 * j + (row >> 5)) * NH + head) * SK + (row & 31)) * (DH * 2) + (cc ^ ((row >> 1) & 7)) * 16;
        }
#pragma unroll
        for (int e = 0; e < 5; ++e) {
            const unsigned p = (L.lw * 5 + e) * 64 + lane, row = p / 20, cc = p % 20;
            L.vO[e] = row * (DM * 2) + i * 320 + (cc ^ (((row >> 3) & 1) << 1)) * 16;
        }
        issue_stage<0>(L); issue_stage<1>(L); issue_stage<2>(L);
        ST(1);
        loader_steps<0>(L, stv, lane);
        ST(53);
    } else {
        const int wb = wave >> 1, wd = wave & 1;
        if (wave == 0) {
            const u64 e = __hip_atomic_load((const gu64*)(a.ws + WS_EPOCH + j * 256), RLX_AGENT) + 1;
            if (lane == 0) *(LAS u64*)(uintptr_t)TAGW = e;
        }
        {
            const int h = lane >> 5, r = lane & 31;
            unsigned aW[3];
            {
                const int grp = (lane >> 4) & 1, q = (lane >> 2) & 3, p = lane & 3;
                const int bs = ((p & 1) << 1) | (p >> 1);
                const int m = (q >> 1) & 1;
#pragma unroll
                for (int t = 0; t < 3; ++t)
                    aW[t] = 8192 + (8 * h + q) * 384 + grp * 32 + bs * 8 + (((3 * wd + t) ^ m) * 64);
            }
            unsigned aX[4];
            {
                const int row = 32 * wb + r;
#pragma unroll
                for (int ks = 0; ks < 4; ++ks) aX[ks] = (row * 128 + ((h ^ ((row >> 1) & 7)) * 16)) ^ (32 * ks);
            }
            f32x16 acc[3];
#pragma unroll
            for (int t = 0; t < 3; ++t)
#pragma unroll
                for (int e = 0; e < 16; ++e) acc[t][e] = 0.f;
            s16x8 A0[2][3], B0[2], A1[2][3], B1[2];
            auto rd = [&](s16x8 (&A)[2][3], s16x8 (&Bf)[2], unsigned so, int ds) {
#pragma unroll
                for (int s = 0; s < 2; ++s) {
                    const int ks = 2 * ds + s;
                    Bf[s] = lds_b128(aX[ks] + so);
#pragma unroll
                    for (int t = 0; t < 3; ++t)
                        A[s][t] = cat(lds_tr(aW[t] + so + ks * 16 * 384), lds_tr(aW[t] + so + (ks * 16 + 4) * 384));
                }
            };
            auto mm = [&](s16x8 (&A)[2][3], s16x8 (&Bf)[2]) {
#pragma unroll
                for (int s = 0; s < 2; ++s)
#pragma unroll
                    for (int t = 0; t < 3; ++t) acc[t] = MFMA32(A[s][t], Bf[s], acc[t]);
            };
            BARRIER_LGKM();
            ST(2);
            SCHED0();
            rd(A0, B0, 0, 0);
            SCHED0();
            rd(A1, B1, 0, 1);
            mm(A0, B0);
            SGB_P1();
            SCHED0();
#pragma unroll 1
            for (int t = 1; t < 10; ++t) {
                const unsigned so = (t & 3) * SLOT;
                BARRIER_LGKM();
                STS(2 + t);
                SCHED0();
                rd(A0, B0, so, 0);
                mm(A1, B1);
                SGB_P1();
                SCHED0();
                rd(A1, B1, so, 1);
                mm(A0, B0);
                SGB_P1();
                SCHED0();
            }
            BARRIER_LGKM();
            ST(12);
            s16x8 Kf[3][2];
            {
                const int row = 32 * wb + r;
                const unsigned kb = (10 % NSLOT) * SLOT + row * 384;
                const int m = (row >> 1) & 7;
#pragma unroll
                for (int t = 0; t < 3; ++t)
#pragma unroll
                    for (int s = 0; s < 2; ++s) {
                        const int c = 12 * wd + 4 * t + 2 * s + h;
                        Kf[t][s] = lds_b128(kb + ((c & ~7) | ((c & 7) ^ m)) * 16);
                    }
            }
            SCHED0();
            mm(A1, B1);
            f32x16 S;
#pragma unroll
            for (int e = 0; e < 16; ++e) S[e] = 0.f;
#pragma unroll
            for (int t = 0; t < 3; ++t)
#pragma unroll
                for (int s = 0; s < 2; ++s) {
                    u32x4 qp;
#pragma unroll
                    for (int e = 0; e < 4; ++e) qp[e] = pk2(acc[t][8 * s + 2 * e], acc[t][8 * s + 2 * e + 1]);
                    S = MFMA32(Kf[t][s], __builtin_bit_cast(s16x8, qp), S);
                }
            const unsigned sx = MISC + wb * 4096 + lane * 16;
            if (wd == 1) {
#pragma unroll
                for (int gq = 0; gq < 4; ++gq) {
                    f32x4 v = {S[4 * gq], S[4 * gq + 1], S[4 * gq + 2], S[4 * gq + 3]};
                    *(LAS f32x4*)(uintptr_t)(sx + gq * 1024) = v;
                }
            }
            ST(40);
            BARRIER_LGKM();
            ST(13);
            if (wd == 0) {
#pragma unroll
                for (int gq = 0; gq < 4; ++gq) {
                    const f32x4 v = *(LAS f32x4*)(uintptr_t)(sx + gq * 1024);
#pragma unroll
                    for (int e = 0; e < 4; ++e) S[4 * gq + e] += v[e];
                }
                const float c = 0.07216878364870322f * 1.4426950408889634f;
                float mx = S[0];
#pragma unroll
                for (int e = 1; e < 16; ++e) mx = fmaxf(mx, S[e]);
                mx = fmaxf(mx, __shfl_xor(mx, 32));
                float sum = 0.f;
#pragma unroll
                for (int e = 0; e < 16; ++e) { S[e] = __builtin_amdgcn_exp2f((S[e] - mx) * c); sum += S[e]; }
                sum += __shfl_xor(sum, 32);
                const float inv = 1.0f / sum;
                u32x4 f0, f1;
#pragma unroll
                for (int e = 0; e < 2; ++e) {
                    f0[e] = pk2(S[2 * e] * inv, S[2 * e + 1] * inv);
                    f0[2 + e] = pk2(S[8 + 2 * e] * inv, S[8 + 2 * e + 1] * inv);
                    f1[e] = pk2(S[4 + 2 * e] * inv, S[4 + 2 * e + 1] * inv);
                    f1[2 + e] = pk2(S[12 + 2 * e] * inv, S[12 + 2 * e + 1] * inv);
                }
                const unsigned pb = WS_P + ((j * NH + head) * 2 + wb) * 2048 + (r >> 4) * 1024 + (r & 15) * 16;
                __builtin_amdgcn_raw_buffer_store_b128(f0, rws, pb + h * 256, 0, 16);
                __builtin_amdgcn_raw_buffer_store_b128(f1, rws, pb + (2 + h) * 256, 0, 16);
            }
            ST(41);
        }
        {
            const int g = lane >> 4, c = lane & 15;
            unsigned aV[2], aO[2];
            {
                const int row = 32 * wb + c;
                aV[0] = row * 128 + ((g ^ ((c >> 1) & 7)) * 16);
                aV[1] = aV[0] ^ 64;
                const int q = (lane >> 2) & 3, p = lane & 3, m = g & 1;
                const unsigned com = 8192 + (8 * g + q) * 320 + p * 8;
                aO[0] = com + ((5 * wd) ^ m) * 32;
                aO[1] = com + ((5 * wd + 1) ^ m) * 32;
            }
            f32x4 acc[2][5];
            u32x4 vw[4][5];
            s16x8 A0[2], B0[5], A1[2], B1[5];
            u32x4 pf[4][2];
            u64 tag = 0, seen = 0;
            const gu64* f1 = (const gu64*)(a.ws + WS_F1 + blockIdx.x * 128) + (lane & 3);
            auto rd = [&](s16x8 (&A)[2], s16x8 (&Bf)[5], unsigned so, int ks) {
#pragma unroll
                for (int kt = 0; kt < 2; ++kt) A[kt] = lds_b128(aV[ks] + so + kt * 2048);
#pragma unroll
                for (int nt = 0; nt < 5; ++nt) {
                    const unsigned ad = aO[nt & 1] + so + (nt & ~1) * 32 + ks * 32 * 320;
                    Bf[nt] = cat(lds_tr(ad), lds_tr(ad + 4 * 320));
                }
            };
            auto mm = [&](s16x8 (&A)[2], s16x8 (&Bf)[5]) {
#pragma unroll
                for (int nt = 0; nt < 5; ++nt)
#pragma unroll
                    for (int kt = 0; kt < 2; ++kt) acc[kt][nt] = MFMA16(A[kt], Bf[nt], acc[kt][nt]);
            };
            auto mm0 = [&](s16x8 (&A)[2], s16x8 (&Bf)[5]) {
#pragma unroll
                for (int nt = 0; nt < 5; ++nt)
#pragma unroll
                    for (int kt = 0; kt < 2; ++kt) acc[kt][nt] = MFMA16(A[kt], Bf[nt], ((f32x4){0.f, 0.f, 0.f, 0.f}));
            };
            auto pack = [&](u32x4 (&o)[5]) {
#pragma unroll
                for (int nt = 0; nt < 5; ++nt) {
                    o[nt][0] = pk2(acc[0][nt][0], acc[0][nt][1]); o[nt][1] = pk2(acc[0][nt][2], acc[0][nt][3]);
                    o[nt][2] = pk2(acc[1][nt][0], acc[1][nt][1]); o[nt][3] = pk2(acc[1][nt][2], acc[1][nt][3]);
                }
            };
#pragma unroll
            for (int hh = 0; hh < 4; ++hh) {
#pragma unroll
                for (int dc = 0; dc < 3; ++dc) {
                    const int t = 11 + 3 * hh + dc;
                    const unsigned so = (t % NSLOT) * SLOT;
                    if (t == 14) asm volatile("s_waitcnt vmcnt(0)" ::: "memory");
                    if (t > 11) BARRIER_LGKM();
                    STS(2 + t);
                    if (t == 14) {
                        tag = *(LAS u64*)(uintptr_t)TAGW;
                        if (wave == 1 && lane < 4)
                            __hip_atomic_store((gu64*)(a.ws + WS_F1 + ((j * 4 + lane) * 8 + xg) * 128) + i, tag, RLX_AGENT);
                    }
                    if (t == 19) seen = __hip_atomic_load(f1, RLX_AGENT);
                    if (t == 20) {
                        wait_tags(f1, tag, true, seen);
#pragma unroll
                        for (int h2 = 0; h2 < 4; ++h2) {
                            const unsigned pb = WS_P + ((j * NH + xg * 4 + h2) * 2 + wb) * 2048 + lane * 16;
                            pf[h2][0] = __builtin_amdgcn_raw_buffer_load_b128(rws, pb, 0, 16);
                            pf[h2][1] = __builtin_amdgcn_raw_buffer_load_b128(rws, pb + 1024, 0, 16);
                        }
                    }
                    SCHED0();
                    rd(A0, B0, so, 0);
                    if (hh + dc > 0) { mm(A1, B1); SGB_P2(); }
                    SCHED0();
                    if (dc == 0 && hh > 0) { pack(vw[hh - 1]); SCHED0(); }
                    rd(A1, B1, so, 1);
                    if (dc == 0) mm0(A0, B0); else mm(A0, B0);
                    SGB_P2();
                    SCHED0();
                }
            }
            mm(A1, B1);
            pack(vw[3]);
            ST(42);
            f32x4 o[5][2];
#pragma unroll
            for (int nt = 0; nt < 5; ++nt) { o[nt][0] = (f32x4){0.f, 0.f, 0.f, 0.f}; o[nt][1] = o[nt][0]; }
#pragma unroll
            for (int hh = 0; hh < 4; ++hh) {
#pragma unroll
                for (int nt = 0; nt < 5; ++nt) {
                    o[nt][0] = MFMA16(__builtin_bit_cast(s16x8, vw[hh][nt]), __builtin_bit_cast(s16x8, pf[hh][0]), o[nt][0]);
                    o[nt][1] = MFMA16(__builtin_bit_cast(s16x8, vw[hh][nt]), __builtin_bit_cast(s16x8, pf[hh][1]), o[nt][1]);
                }
            }
            ST(43);
            const unsigned pp = WS_PART + (((j * 4 + i) * 8 + xg) * 8) * 5120 + (wb * 4 + (c >> 3)) * 5120 + wd * 2560 + g * 128 + (c & 7) * 16;
#pragma unroll
            for (int nt = 0; nt < 5; ++nt)
#pragma unroll
                for (int qt = 0; qt < 2; ++qt)
                    __builtin_amdgcn_raw_buffer_store_b128(__builtin_bit_cast(u32x4, o[nt][qt]), rws, pp + qt * 2 * 5120 + nt * 512, 0, 16);
            ST(44);
        }
    }
    BARRIER_VM0();
    ST(45);
    if (wave == 4) {
        const u64 tag = *(LAS u64*)(uintptr_t)TAGW;
        if (lane < 8) __hip_atomic_store((gu64*)(a.ws + WS_F2 + ((j * 4 + i) * 8 + lane) * 128) + xg, tag, RLX_AGENT);
        const gu64* f2 = (const gu64*)(a.ws + WS_F2 + blockIdx.x * 128) + (lane & 7);
        wait_tags(f2, tag, true, __hip_atomic_load(f2, RLX_AGENT));
        if (xg == 0 && i == 0 && lane == 0) __hip_atomic_store((gu64*)(a.ws + WS_EPOCH + j * 256), tag, RLX_AGENT);
    }
    asm volatile("s_barrier" ::: "memory");
    ST(46);
    if (wave < 5) {
        const int L = tid;
        const unsigned pp = WS_PART + (((j * 4 + i) * 8) * 8 + xg) * 5120 + L * 16;
        f32x4 v[8];
#pragma unroll
        for (int s = 0; s < 8; ++s) v[s] = __builtin_bit_cast(f32x4, __builtin_amdgcn_raw_buffer_load_b128(rws, pp + s * 8 * 5120, 0, 16));
        const f32x4 t = ((v[0] + v[1]) + (v[2] + v[3])) + ((v[4] + v[5]) + (v[6] + v[7]));
        const int cl = L & 7, g = (L >> 3) & 3, nn = L >> 5;
        const int row = 64 * j + 8 * xg + cl, n = 160 * i + 16 * nn + 4 * g;
        const u32x2 o = {pk2(t[0], t[1]), pk2(t[2], t[3])};
        asm volatile("" ::: "memory");
        ST(47);
        *(u32x2*)((char*)a.out + ((size_t)row * DM + n) * 2) = o;
    }
}

extern "C" void kernel_launch(void* const* d_in, const int* in_sizes, int n_in, void* d_out, int out_size,
                              void* d_ws, size_t ws_size, hipStream_t stream) {
    Args a;
    a.x = d_in[0]; a.kc = d_in[1]; a.vc = d_in[2]; a.wq = d_in[3]; a.wo = d_in[4];
    a.out = d_out; a.ws = (char*)d_ws;
    gqa_fused<<<256, 512, 0, stream>>>(a);
}
```

```cpp
#define OUTAUX 2
#define PADTICKS 3000
#include <hip/hip_runtime.h>
#include <hip/hip_bf16.h>
#include <stdint.h>

#define LAS __attribute__((address_space(3)))
#define GAS __attribute__((address_space(1)))
#define DI __device__ __forceinline__
typedef short s16x4 __attribute__((ext_vector_type(4)));
typedef short s16x8 __attribute__((ext_vector_type(8)));
typedef __bf16 bf16x8 __attribute__((ext_vector_type(8)));
typedef __bf16 bf16x2 __attribute__((ext_vector_type(2)));
typedef float f32x2 __attribute__((ext_vector_type(2)));
typedef float f32x4 __attribute__((ext_vector_type(4)));
typedef float f32x16 __attribute__((ext_vector_type(16)));
typedef unsigned u32x2 __attribute__((ext_vector_type(2)));
typedef unsigned u32x4 __attribute__((ext_vector_type(4)));
typedef unsigned long long u64;
typedef GAS u64 gu64;

constexpr int NB = 16, SQ = 32, DM = 640, NH = 32, SK = 32, DH = 192, HD = NH * DH;
constexpr int SLOT = 32768, NSLOT = 4;
constexpr int MISC = SLOT * NSLOT;
constexpr int TAGW = MISC + 8192;
constexpr int LDS_BYTES = TAGW + 64;
constexpr unsigned WS_EPOCH = 0;
constexpr unsigned WS_F1 = 4096;
constexpr unsigned WS_F2 = WS_F1 + 256 * 128;
constexpr unsigned WS_P = 1 << 17;
constexpr unsigned WS_P_BYTES = 8 * 32 * 2 * 2048;
constexpr unsigned WS_PART = WS_P + WS_P_BYTES;
constexpr unsigned WS_PART_BYTES = 32 * 8 * 8 * 5120;
constexpr unsigned WS_USED = WS_PART + WS_PART_BYTES;
constexpr unsigned kSpinCap = 1u << 18;

struct Args {
    const void* x; const void* kc; const void* vc; const void* wq; const void* wo;
    void* out; char* ws;
};

__shared__ __attribute__((aligned(16))) char smem[LDS_BYTES];

DI bf16x8 as_bf(s16x8 v) { return __builtin_bit_cast(bf16x8, v); }
DI unsigned pk2(float a, float b) {
    f32x2 v = {a, b};
    bf16x2 r = __builtin_convertvector(v, bf16x2);
    return __builtin_bit_cast(unsigned, r);
}
DI s16x8 lds_b128(unsigned addr) { return *(LAS s16x8*)(uintptr_t)addr; }
DI s16x4 lds_tr(unsigned addr) { return __builtin_amdgcn_ds_read_tr16_b64_v4i16((LAS s16x4*)(uintptr_t)addr); }
DI s16x8 cat(s16x4 a, s16x4 b) { return __builtin_shufflevector(a, b, 0, 1, 2, 3, 4, 5, 6, 7); }
#define MFMA32(a, b, c) __builtin_amdgcn_mfma_f32_32x32x16_bf16(as_bf(a), as_bf(b), (c), 0, 0, 0)
#define MFMA16(a, b, c) __builtin_amdgcn_mfma_f32_16x16x32_bf16(as_bf(a), as_bf(b), (c), 0, 0, 0)
#define BARRIER_LGKM() asm volatile("s_waitcnt lgkmcnt(0)\n\ts_barrier" ::: "memory")
#define BARRIER_VM0() asm volatile("s_waitcnt vmcnt(0) lgkmcnt(0)\n\ts_barrier" ::: "memory")
#define SCHED0() __builtin_amdgcn_sched_barrier(0)
#define SGB(m, n) __builtin_amdgcn_sched_group_barrier(m, n, 0)
#define SGB_P1() do { SGB(0x8, 1); SGB(0x100, 5); SGB(0x8, 1); SGB(0x100, 5); SGB(0x8, 1); SGB(0x100, 4); SGB(0x8, 3); } while (0)
#define SGB_P2() do { SGB(0x8, 1); SGB(0x100, 2); SGB(0x8, 1); SGB(0x100, 2); SGB(0x8, 1); SGB(0x100, 2); SGB(0x8, 1); SGB(0x100, 2); \
                      SGB(0x8, 1); SGB(0x100, 2); SGB(0x8, 1); SGB(0x100, 2); SGB(0x8, 4); } while (0)
#define RLX_AGENT __ATOMIC_RELAXED, __HIP_MEMORY_SCOPE_AGENT

#define STAMP_DECL
#define ST(id)
#define STS(id)
#define STR(id)
#define ST_FLUSH(role)

DI void wait_tags(const gu64* word, u64 tag, bool active, u64 first) {
    u64 v = first;
    for (unsigned spins = 0; !__all(!active || v == tag); ++spins) {
        if (spins > kSpinCap) break;
        __builtin_amdgcn_s_sleep(1);
        if (active) v = __hip_atomic_load(word, RLX_AGENT);
    }
}

constexpr int T1 = 23;
constexpr int n_dma(int t) { return t < 0 ? 0 : t < 10 ? 8 : t == 10 ? 6 : t < T1 ? 7 : 0; }

template <int N> DI void wait_vm_barrier() {
    asm volatile("s_waitcnt vmcnt(%0)\n\ts_barrier" ::"n"(N) : "memory");
}

struct Loader {
    __amdgpu_buffer_rsrc_t rx, rk, rv, rwq, rwo;
    unsigned vX[2], vW[6], vK[6], vV[2], vO[5];
    unsigned lw;
    unsigned xg;
};

template <int T> DI void issue_stage(const Loader& L) {
    LAS char* base = (LAS char*)smem + (T % NSLOT) * SLOT;
    if constexpr (T < 10) {
#pragma unroll
        for (int e = 0; e < 2; ++e)
            __builtin_amdgcn_raw_ptr_buffer_load_lds(L.rx, (LAS void*)(base + (L.lw * 2 + e) * 1024), 16, L.vX[e], T * 128, 0, 0);
#pragma unroll
        for (int e = 0; e < 6; ++e)
            __builtin_amdgcn_raw_ptr_buffer_load_lds(L.rwq, (LAS void*)(base + 8192 + (L.lw * 6 + e) * 1024), 16, L.vW[e], T * 64 * (HD * 2), 0, 0);
    } else if constexpr (T == 10) {
#pragma unroll
        for (int e = 0; e < 6; ++e)
            __builtin_amdgcn_raw_ptr_buffer_load_lds(L.rk, (LAS void*)(base + (L.lw * 6 + e) * 1024), 16, L.vK[e], 0, 0, 0);
    } else if constexpr (T < T1) {
        constexpr int u = T - 11, hh = u / 3, dc = u % 3;
        const unsigned sv = (L.xg * 4 + hh) * (SK * DH * 2) + dc * 128;
        const unsigned so = ((L.xg * 4 + hh) * DH + dc * 64) * (DM * 2);
#pragma unroll
        for (int e = 0; e < 2; ++e)
            __builtin_amdgcn_raw_ptr_buffer_load_lds(L.rv, (LAS void*)(base + (L.lw * 2 + e) * 1024), 16, L.vV[e], sv, 0, 0);
#pragma unroll
        for (int e = 0; e < 5; ++e)
            __builtin_amdgcn_raw_ptr_buffer_load_lds(L.rwo, (LAS void*)(base + 8192 + (L.lw * 5 + e) * 1024), 16, L.vO[e], so, 0, 0);
    }
}

template <int T> DI void loader_steps(const Loader& L, unsigned& stv, int lane) {
    if constexpr (T < T1) {
        constexpr int pend = n_dma(T + 1) + n_dma(T + 2);
        wait_vm_barrier<pend>();
        STS(2 + T);
        if constexpr (T + 3 < T1) issue_stage<T + 3>(L);
        STS(30 + T);
        loader_steps<T + 1>(L, stv, lane);
    }
}

__global__ void __launch_bounds__(512, 2) gqa_fused(Args a) {
    const int tid = threadIdx.x;
    const int lane = tid & 63;
    const int wave = __builtin_amdgcn_readfirstlane(tid >> 6);
    const int xg = blockIdx.x & 7, loc = blockIdx.x >> 3;
    const int j = loc >> 2, i = loc & 3;
    const int head = xg * 4 + i;
    const __amdgpu_buffer_rsrc_t rws = __builtin_amdgcn_make_buffer_rsrc((void*)a.ws, 0, WS_USED, 0x00020000);
    unsigned long long pad_t0; asm volatile("s_memrealtime %0\n\ts_waitcnt lgkmcnt(0)" : "=s"(pad_t0) :: "memory");
    STAMP_DECL; STR(62); ST(0);
    unsigned stv = 0;

    if (wave >= 4) {
        Loader L;
        L.lw = wave - 4; L.xg = xg;
        L.rx = __builtin_amdgcn_make_buffer_rsrc((void*)a.x, 0, NB * SQ * DM * 2, 0x00020000);
        L.rk = __builtin_amdgcn_make_buffer_rsrc((void*)a.kc, 0, NB * NH * SK * DH * 2, 0x00020000);
        L.rv = __builtin_amdgcn_make_buffer_rsrc((void*)a.vc, 0, NB * NH * SK * DH * 2, 0x00020000);
        L.rwq = __builtin_amdgcn_make_buffer_rsrc((void*)a.wq, 0, DM * HD * 2, 0x00020000);
        L.rwo = __builtin_amdgcn_make_buffer_rsrc((void*)a.wo, 0, HD * DM * 2, 0x00020000);
#pragma unroll
        for (int e = 0; e < 2; ++e) {
            const unsigned p = (L.lw * 2 + e) * 64 + lane, row = p >> 3, c = (p & 7) ^ ((row >> 1) & 7);
            L.vX[e] = (64 * j + row) * (DM * 2) + c * 16;
            L.vV[e] = ((2 * j + (row >> 5)) * NH * SK + (row & 31)) * (DH * 2) + c * 16;
        }
#pragma unroll
        for (int e = 0; e < 6; ++e) {
            const unsigned p = (L.lw * 6 + e) * 64 + lane, row = p / 24, cc = p % 24;
            L.vW[e] = row * (HD * 2) + head * (DH * 2) + (cc ^ (((row >> 1) & 1) << 2)) * 16;
            L.vK[e] = (((2 * j + (row >> 5)) * NH + head) * SK + (row & 31)) * (DH * 2) + (cc ^ ((row >> 1) & 7)) * 16;
        }
#pragma unroll
        for (int e = 0; e < 5; ++e) {
            const unsigned p = (L.lw * 5 + e) * 64 + lane, row = p / 20, cc = p % 20;
            L.vO[e] = row * (DM * 2) + i * 320 + (cc ^ (((row >> 3) & 1) << 1)) * 16;
        }
        issue_stage<0>(L); issue_stage<1>(L); issue_stage<2>(L);
        ST(1);
        loader_steps<0>(L, stv, lane);
        ST(53);
    } else {
        const int wb = wave >> 1, wd = wave & 1;
        if (wave == 0) {
            const u64 e = __hip_atomic_load((const gu64*)(a.ws + WS_EPOCH + j * 256), RLX_AGENT) + 1;
            if (lane == 0) *(LAS u64*)(uintptr_t)TAGW = e;
        }
        {
            const int h = lane >> 5, r = lane & 31;
            unsigned aW[3];
            {
                const int grp = (lane >> 4) & 1, q = (lane >> 2) & 3, p = lane & 3;
                const int bs = ((p & 1) << 1) | (p >> 1);
                const int m = (q >> 1) & 1;
#pragma unroll
                for (int t = 0; t < 3; ++t)
                    aW[t] = 8192 + (8 * h + q) * 384 + grp * 32 + bs * 8 + (((3 * wd + t) ^ m) * 64);
            }
            unsigned aX[4];
            {
                const int row = 32 * wb + r;
#pragma unroll
                for (int ks = 0; ks < 4; ++ks) aX[ks] = (row * 128 + ((h ^ ((row >> 1) & 7)) * 16)) ^ (32 * ks);
            }
            f32x16 acc[3];
#pragma unroll
            for (int t = 0; t < 3; ++t)
#pragma unroll
                for (int e = 0; e < 16; ++e) acc[t][e] = 0.f;
            s16x8 A0[2][3], B0[2], A1[2][3], B1[2];
            auto rd = [&](s16x8 (&A)[2][3], s16x8 (&Bf)[2], unsigned so, int ds) {
#pragma unroll
                for (int s = 0; s < 2; ++s) {
                    const int ks = 2 * ds + s;
                    Bf[s] = lds_b128(aX[ks] + so);
#pragma unroll
                    for (int t = 0; t < 3; ++t)
                        A[s][t] = cat(lds_tr(aW[t] + so + ks * 16 * 384), lds_tr(aW[t] + so + (ks * 16 + 4) * 384));
                }
            };
            auto mm = [&](s16x8 (&A)[2][3], s16x8 (&Bf)[2]) {
#pragma unroll
                for (int s = 0; s < 2; ++s)
#pragma unroll
                    for (int t = 0; t < 3; ++t) acc[t] = MFMA32(A[s][t], Bf[s], acc[t]);
            };
            BARRIER_LGKM();
            ST(2);
            SCHED0();
            rd(A0, B0, 0, 0);
            SCHED0();
            rd(A1, B1, 0, 1);
            mm(A0, B0);
            SGB_P1();
            SCHED0();
#pragma unroll 1
            for (int t = 1; t < 10; ++t) {
                const unsigned so = (t & 3) * SLOT;
                BARRIER_LGKM();
                STS(2 + t);
                SCHED0();
                rd(A0, B0, so, 0);
                mm(A1, B1);
                SGB_P1();
                SCHED0();
                rd(A1, B1, so, 1);
                mm(A0, B0);
                SGB_P1();
                SCHED0();
            }
            BARRIER_LGKM();
            ST(12);
            s16x8 Kf[3][2];
            {
                const int row = 32 * wb + r;
                const unsigned kb = (10 % NSLOT) * SLOT + row * 384;
                const int m = (row >> 1) & 7;
#pragma unroll
                for (int t = 0; t < 3; ++t)
#pragma unroll
                    for (int s = 0; s < 2; ++s) {
                        const int c = 12 * wd + 4 * t + 2 * s + h;
                        Kf[t][s] = lds_b128(kb + ((c & ~7) | ((c & 7) ^ m)) * 16);
                    }
            }
            SCHED0();
            mm(A1, B1);
            f32x16 S;
#pragma unroll
            for (int e = 0; e < 16; ++e) S[e] = 0.f;
#pragma unroll
            for (int t = 0; t < 3; ++t)
#pragma unroll
                for (int s = 0; s < 2; ++s) {
                    u32x4 qp;
#pragma unroll
                    for (int e = 0; e < 4; ++e) qp[e] = pk2(acc[t][8 * s + 2 * e], acc[t][8 * s + 2 * e + 1]);
                    S = MFMA32(Kf[t][s], __builtin_bit_cast(s16x8, qp), S);
                }
            const unsigned sx = MISC + wb * 4096 + lane * 16;
            if (wd == 1) {
#pragma unroll
                for (int gq = 0; gq < 4; ++gq) {
                    f32x4 v = {S[4 * gq], S[4 * gq + 1], S[4 * gq + 2], S[4 * gq + 3]};
                    *(LAS f32x4*)(uintptr_t)(sx + gq * 1024) = v;
                }
            }
            ST(40);
            BARRIER_LGKM();
            ST(13);
            if (wd == 0) {
#pragma unroll
                for (int gq = 0; gq < 4; ++gq) {
                    const f32x4 v = *(LAS f32x4*)(uintptr_t)(sx + gq * 1024);
#pragma unroll
                    for (int e = 0; e < 4; ++e) S[4 * gq + e] += v[e];
                }
                const float c = 0.07216878364870322f * 1.4426950408889634f;
                float mx = S[0];
#pragma unroll
                for (int e = 1; e < 16; ++e) mx = fmaxf(mx, S[e]);
                mx = fmaxf(mx, __shfl_xor(mx, 32));
                float sum = 0.f;
#pragma unroll
                for (int e = 0; e < 16; ++e) { S[e] = __builtin_amdgcn_exp2f((S[e] - mx) * c); sum += S[e]; }
                sum += __shfl_xor(sum, 32);
                const float inv = 1.0f / sum;
                u32x4 f0, f1;
#pragma unroll
                for (int e = 0; e < 2; ++e) {
                    f0[e] = pk2(S[2 * e] * inv, S[2 * e + 1] * inv);
                    f0[2 + e] = pk2(S[8 + 2 * e] * inv, S[8 + 2 * e + 1] * inv);
                    f1[e] = pk2(S[4 + 2 * e] * inv, S[4 + 2 * e + 1] * inv);
                    f1[2 + e] = pk2(S[12 + 2 * e] * inv, S[12 + 2 * e + 1] * inv);
                }
                const unsigned pb = WS_P + ((j * NH + head) * 2 + wb) * 2048 + (r >> 4) * 1024 + (r & 15) * 16;
                __builtin_amdgcn_raw_buffer_store_b128(f0, rws, pb + h * 256, 0, 16);
                __builtin_amdgcn_raw_buffer_store_b128(f1, rws, pb + (2 + h) * 256, 0, 16);
            }
            ST(41);
        }
        {
            const int g = lane >> 4, c = lane & 15;
            unsigned aV[2], aO[2];
            {
                const int row = 32 * wb + c;
                aV[0] = row * 128 + ((g ^ ((c >> 1) & 7)) * 16);
                aV[1] = aV[0] ^ 64;
                const int q = (lane >> 2) & 3, p = lane & 3, m = g & 1;
                const unsigned com = 8192 + (8 * g + q) * 320 + p * 8;
                aO[0] = com + ((5 * wd) ^ m) * 32;
                aO[1] = com + ((5 * wd + 1) ^ m) * 32;
            }
            f32x4 acc[2][5];
            u32x4 vw[4][5];
            s16x8 A0[2], B0[5], A1[2], B1[5];
            u32x4 pf[4][2];
            u64 tag = 0, seen = 0;
            const gu64* f1 = (const gu64*)(a.ws + WS_F1 + blockIdx.x * 128) + (lane & 3);
            auto rd = [&](s16x8 (&A)[2], s16x8 (&Bf)[5], unsigned so, int ks) {
#pragma unroll
                for (int kt = 0; kt < 2; ++kt) A[kt] = lds_b128(aV[ks] + so + kt * 2048);
#pragma unroll
                for (int nt = 0; nt < 5; ++nt) {
                    const unsigned ad = aO[nt & 1] + so + (nt & ~1) * 32 + ks * 32 * 320;
                    Bf[nt] = cat(lds_tr(ad), lds_tr(ad + 4 * 320));
                }
            };
            auto mm = [&](s16x8 (&A)[2], s16x8 (&Bf)[5]) {
#pragma unroll
                for (int nt = 0; nt < 5; ++nt)
#pragma unroll
                    for (int kt = 0; kt < 2; ++kt) acc[kt][nt] = MFMA16(A[kt], Bf[nt], acc[kt][nt]);
            };
            auto mm0 = [&](s16x8 (&A)[2], s16x8 (&Bf)[5]) {
#pragma unroll
                for (int nt = 0; nt < 5; ++nt)
#pragma unroll
                    for (int kt = 0; kt < 2; ++kt) acc[kt][nt] = MFMA16(A[kt], Bf[nt], ((f32x4){0.f, 0.f, 0.f, 0.f}));
            };
            auto pack = [&](u32x4 (&o)[5]) {
#pragma unroll
                for (int nt = 0; nt < 5; ++nt) {
                    o[nt][0] = pk2(acc[0][nt][0], acc[0][nt][1]); o[nt][1] = pk2(acc[0][nt][2], acc[0][nt][3]);
                    o[nt][2] = pk2(acc[1][nt][0], acc[1][nt][1]); o[nt][3] = pk2(acc[1][nt][2], acc[1][nt][3]);
                }
            };
#pragma unroll
            for (int hh = 0; hh < 4; ++hh) {
#pragma unroll
                for (int dc = 0; dc < 3; ++dc) {
                    const int t = 11 + 3 * hh + dc;
                    const unsigned so = (t % NSLOT) * SLOT;
                    if (t == 14) asm volatile("s_waitcnt vmcnt(0)" ::: "memory");
                    if (t > 11) BARRIER_LGKM();
                    STS(2 + t);
                    if (t == 14) {
                        tag = *(LAS u64*)(uintptr_t)TAGW;
                        if (wave == 1 && lane < 4)
                            __hip_atomic_store((gu64*)(a.ws + WS_F1 + ((j * 4 + lane) * 8 + xg) * 128) + i, tag, RLX_AGENT);
                    }
                    if (t == 19) seen = __hip_atomic_load(f1, RLX_AGENT);
                    if (t == 20) {
                        wait_tags(f1, tag, true, seen);
#pragma unroll
                        for (int h2 = 0; h2 < 4; ++h2) {
                            const unsigned pb = WS_P + ((j * NH + xg * 4 + h2) * 2 + wb) * 2048 + lane * 16;
                            pf[h2][0] = __builtin_amdgcn_raw_buffer_load_b128(rws, pb, 0, 16);
                            pf[h2][1] = __builtin_amdgcn_raw_buffer_load_b128(rws, pb + 1024, 0, 16);
                        }
                    }
                    SCHED0();
                    rd(A0, B0, so, 0);
                    if (hh + dc > 0) { mm(A1, B1); SGB_P2(); }
                    SCHED0();
                    if (dc == 0 && hh > 0) { pack(vw[hh - 1]); SCHED0(); }
                    rd(A1, B1, so, 1);
                    if (dc == 0) mm0(A0, B0); else mm(A0, B0);
                    SGB_P2();
                    SCHED0();
                }
            }
            mm(A1, B1);
            pack(vw[3]);
            ST(42);
            f32x4 o[5][2];
#pragma unroll
            for (int nt = 0; nt < 5; ++nt) { o[nt][0] = (f32x4){0.f, 0.f, 0.f, 0.f}; o[nt][1] = o[nt][0]; }
#pragma unroll
            for (int hh = 0; hh < 4; ++hh) {
#pragma unroll
                for (int nt = 0; nt < 5; ++nt) {
                    o[nt][0] = MFMA16(__builtin_bit_cast(s16x8, vw[hh][nt]), __builtin_bit_cast(s16x8, pf[hh][0]), o[nt][0]);
                    o[nt][1] = MFMA16(__builtin_bit_cast(s16x8, vw[hh][nt]), __builtin_bit_cast(s16x8, pf[hh][1]), o[nt][1]);
                }
            }
            ST(43);
            const unsigned pp = WS_PART + (((j * 4 + i) * 8 + xg) * 8) * 5120 + (wb * 4 + (c >> 3)) * 5120 + wd * 2560 + g * 128 + (c & 7) * 16;
#pragma unroll
            for (int nt = 0; nt < 5; ++nt)
#pragma unroll
                for (int qt = 0; qt < 2; ++qt)
                    __builtin_amdgcn_raw_buffer_store_b128(__builtin_bit_cast(u32x4, o[nt][qt]), rws, pp + qt * 2 * 5120 + nt * 512, 0, 16);
            ST(44);
        }
    }
    BARRIER_VM0();
    ST(45);
    if (wave == 4) {
        const u64 tag = *(LAS u64*)(uintptr_t)TAGW;
        if (lane < 8) __hip_atomic_store((gu64*)(a.ws + WS_F2 + ((j * 4 + i) * 8 + lane) * 128) + xg, tag, RLX_AGENT);
        const gu64* f2 = (const gu64*)(a.ws + WS_F2 + blockIdx.x * 128) + (lane & 7);
        wait_tags(f2, tag, true, __hip_atomic_load(f2, RLX_AGENT));
        if (xg == 0 && i == 0 && lane == 0) __hip_atomic_store((gu64*)(a.ws + WS_EPOCH + j * 256), tag, RLX_AGENT);
    }
    asm volatile("s_barrier" ::: "memory");
    ST(46);
    if (wave < 5) {
        const int L = tid;
        const unsigned pp = WS_PART + (((j * 4 + i) * 8) * 8 + xg) * 5120 + L * 16;
        f32x4 v[8];
#pragma unroll
        for (int s = 0; s < 8; ++s) v[s] = __builtin_bit_cast(f32x4, __builtin_amdgcn_raw_buffer_load_b128(rws, pp + s * 8 * 5120, 0, 16));
        const f32x4 t = ((v[0] + v[1]) + (v[2] + v[3])) + ((v[4] + v[5]) + (v[6] + v[7]));
        const int cl = L & 7, g = (L >> 3) & 3, nn = L >> 5;
        const int row = 64 * j + 8 * xg + cl, n = 160 * i + 16 * nn + 4 * g;
        const u32x2 o = {pk2(t[0], t[1]), pk2(t[2], t[3])};
        asm volatile("" ::: "memory");
        ST(47);
        { const __amdgpu_buffer_rsrc_t ro = __builtin_amdgcn_make_buffer_rsrc(a.out, 0, NB * SQ * DM * 2, 0x00020000);
          __builtin_amdgcn_raw_buffer_store_b64(o, ro, (row * DM + n) * 2, 0, OUTAUX); }
    }
    { unsigned long long t; do { asm volatile("s_memrealtime %0\n\ts_waitcnt lgkmcnt(0)" : "=s"(t) :: "memory"); } while ((long long)(t - pad_t0) < PADTICKS); }
}

extern "C" void kernel_launch(void* const* d_in, const int* in_sizes, int n_in, void* d_out, int out_size,
                              void* d_ws, size_t ws_size, hipStream_t stream) {
    Args a;
    a.x = d_in[0]; a.kc = d_in[1]; a.vc = d_in[2]; a.wq = d_in[3]; a.wo = d_in[4];
    a.out = d_out; a.ws = (char*)d_ws;
    gqa_fused<<<256, 512, 0, stream>>>(a);
}
```

```cpp
#include <hip/hip_runtime.h>
#include <hip/hip_bf16.h>
#include <stdint.h>

#define LAS __attribute__((address_space(3)))
#define GAS __attribute__((address_space(1)))
#define DI __device__ __forceinline__
typedef short s16x4 __attribute__((ext_vector_type(4)));
typedef short s16x8 __attribute__((ext_vector_type(8)));
typedef __bf16 bf16x8 __attribute__((ext_vector_type(8)));
typedef __bf16 bf16x2 __attribute__((ext_vector_type(2)));
typedef float f32x2 __attribute__((ext_vector_type(2)));
typedef float f32x4 __attribute__((ext_vector_type(4)));
typedef float f32x16 __attribute__((ext_vector_type(16)));
typedef unsigned u32x2 __attribute__((ext_vector_type(2)));
typedef unsigned u32x4 __attribute__((ext_vector_type(4)));
typedef unsigned long long u64;
typedef GAS u64 gu64;

constexpr int NB = 16, SQ = 32, DM = 640, NH = 32, SK = 32, DH = 192, HD = NH * DH;
constexpr int PK0 = 3, PV0 = 6, PMOD = 2;
constexpr int POLL_T = 19, PLOAD_T = 21;
constexpr int SLOT = 32768, NSLOT = 3;
constexpr int KREG = SLOT * NSLOT;
constexpr int SREG = KREG;
constexpr int V0REG = KREG + 24576;
constexpr int TAGW = V0REG + 24576;
constexpr int LDS_BYTES = TAGW + 64;
constexpr unsigned WS_EPOCH = 0;
constexpr unsigned WS_F1 = 4096;
constexpr unsigned WS_F2 = WS_F1 + 256 * 128;
constexpr unsigned WS_P = 1 << 17;
constexpr unsigned WS_P_BYTES = 8 * 32 * 2 * 2048;
constexpr unsigned WS_PART = WS_P + WS_P_BYTES;
constexpr unsigned PART_CH = 2560;
constexpr unsigned WS_PART_BYTES = 32 * 8 * 8 * PART_CH;
constexpr unsigned WS_USED = WS_PART + WS_PART_BYTES;
constexpr unsigned kSpinCap = 1u << 18;

struct Args {
    const void* x; const void* kc; const void* vc; const void* wq; const void* wo;
    void* out; char* ws;
};

__shared__ __attribute__((aligned(16))) char smem[LDS_BYTES];

DI bf16x8 as_bf(s16x8 v) { return __builtin_bit_cast(bf16x8, v); }
DI unsigned pk2(float a, float b) {
    f32x2 v = {a, b};
    bf16x2 r = __builtin_convertvector(v, bf16x2);
    return __builtin_bit_cast(unsigned, r);
}
DI float bflo(unsigned u) { return __builtin_bit_cast(float, u << 16); }
DI float bfhi(unsigned u) { return __builtin_bit_cast(float, u & 0xffff0000u); }
DI s16x8 lds_b128(unsigned addr) { return *(LAS s16x8*)(uintptr_t)addr; }
DI s16x4 lds_tr(unsigned addr) { return __builtin_amdgcn_ds_read_tr16_b64_v4i16((LAS s16x4*)(uintptr_t)addr); }
DI s16x8 cat(s16x4 a, s16x4 b) { return __builtin_shufflevector(a, b, 0, 1, 2, 3, 4, 5, 6, 7); }
DI void half_pair(float v, float& a, float& b) {
    unsigned u = __builtin_bit_cast(unsigned, v), w = u;
    asm("" : "+v"(w));
    const auto r = __builtin_amdgcn_permlane32_swap(u, w, false, false);
    const unsigned r0 = r[0], r1 = r[1];
    a = __builtin_bit_cast(float, r0); b = __builtin_bit_cast(float, r1);
}
DI float half_max(float v) { float a, b; half_pair(v, a, b); return fmaxf(a, b); }
DI float half_sum(float v) { float a, b; half_pair(v, a, b); return a + b; }
#define MFMA32(a, b, c) __builtin_amdgcn_mfma_f32_32x32x16_bf16(as_bf(a), as_bf(b), (c), 0, 0, 0)
#define MFMA16(a, b, c) __builtin_amdgcn_mfma_f32_16x16x32_bf16(as_bf(a), as_bf(b), (c), 0, 0, 0)
#define BARRIER() asm volatile("s_barrier" ::: "memory")
#define BARRIER_LGKM() asm volatile("s_waitcnt lgkmcnt(0)\n\ts_barrier" ::: "memory")
#define BARRIER_VM0() asm volatile("s_waitcnt vmcnt(0) lgkmcnt(0)\n\ts_barrier" ::: "memory")
#define SCHED0() __builtin_amdgcn_sched_barrier(0)
#define SGB(m, n) __builtin_amdgcn_sched_group_barrier(m, n, 0)
#define SGB_P1() do { SGB(0x8, 1); SGB(0x100, 5); SGB(0x8, 1); SGB(0x100, 5); SGB(0x8, 1); SGB(0x100, 4); SGB(0x8, 3); } while (0)
#define SGB_P2() do { SGB(0x8, 1); SGB(0x100, 2); SGB(0x8, 1); SGB(0x100, 2); SGB(0x8, 1); SGB(0x100, 2); SGB(0x8, 1); SGB(0x100, 2); \
                      SGB(0x8, 1); SGB(0x100, 2); SGB(0x8, 1); SGB(0x100, 2); SGB(0x8, 4); } while (0)
#define RLX_AGENT __ATOMIC_RELAXED, __HIP_MEMORY_SCOPE_AGENT

DI void wait_tags(const gu64* word, u64 tag, u64 first) {
    u64 v = first;
    for (unsigned spins = 0; !__all(v == tag); ++spins) {
        if (spins > kSpinCap) break;
        v = __hip_atomic_load(word, RLX_AGENT);
    }
}

constexpr int T1 = 22;
template <int N> DI void wait_vm_barrier() {
    asm volatile("s_waitcnt vmcnt(%0)\n\ts_barrier" ::"n"(N) : "memory");
}
#define DMA(rsrc, ldsoff, voff, soff) \
    __builtin_amdgcn_raw_ptr_buffer_load_lds(rsrc, (LAS void*)((LAS char*)smem + (ldsoff)), 16, (voff), (soff), 0, 0)

DI int rot_heads(int j, int i) { return (i + j) & 3; }
struct Loader {
    __amdgpu_buffer_rsrc_t rx, rk, rv, rwq, rwo;
    unsigned vX[2], vW[3], vKV[2], vO[5];
    unsigned lw;
    unsigned sX, sW, sK, sV, sO;
    unsigned r1, r2;
};
DI void issue_p1(const Loader& L, unsigned pos, unsigned base) {
    unsigned t = pos + L.r1; t = t >= 10 ? t - 10 : t;
#pragma unroll
    for (int e = 0; e < 4; ++e) DMA(L.rx, base + (L.lw * 4 + e) * 1024, L.vX[e & 1], L.sX + t * 128 + e * 8 * (DM * 2));
#pragma unroll
    for (int e = 0; e < 12; ++e) DMA(L.rwq, base + 8192 + (L.lw * 12 + e) * 1024, L.vW[e % 3], L.sW + t * 64 * (HD * 2) + (e / 3) * 8 * (HD * 2));
}
DI void issue_tile(const Loader& L, __amdgpu_buffer_rsrc_t r, unsigned base, unsigned src) {
#pragma unroll
    for (int e = 0; e < 12; ++e) DMA(r, base + (e >> 2) * 8192 + (L.lw * 4 + (e & 3)) * 1024, L.vKV[e & 1], src + (e >> 2) * 128 + (e & 3) * 8 * (DH * 2));
}
template <bool WITH_V> DI void issue_p2(const Loader& L, unsigned base, unsigned hp, unsigned dc) {
    const unsigned hh = (hp + L.r2) & 3, u = hh * 3 + dc;
    if constexpr (WITH_V) {
#pragma unroll
        for (int e = 0; e < 4; ++e) DMA(L.rv, base + (L.lw * 4 + e) * 1024, L.vKV[e & 1], L.sV + hh * (SK * DH * 2) + dc * 128 + e * 8 * (DH * 2));
    }
#pragma unroll
    for (int e = 0; e < 10; ++e) DMA(L.rwo, base + 8192 + (L.lw * 10 + e) * 1024, L.vO[e % 5], L.sO + u * 64 * (DM * 2) + (e / 5) * 16 * (DM * 2));
}

__global__ void __launch_bounds__(512, 2) gqa_fused(Args a) {
    const int tid = threadIdx.x;
    const int lane = tid & 63;
    const int wave = __builtin_amdgcn_readfirstlane(tid >> 6);
    const int xg = blockIdx.x & 7, loc = blockIdx.x >> 3;
    const int j = loc >> 2, i = loc & 3;
    const int head = xg * 4 + i;
    asm volatile("" ::"s"(a.x), "s"(a.kc), "s"(a.vc), "s"(a.wq), "s"(a.wo), "s"(a.out), "s"(a.ws));
    const __amdgpu_buffer_rsrc_t rws = __builtin_amdgcn_make_buffer_rsrc((void*)a.ws, 0, WS_USED, 0x00020000);

    if (__builtin_expect(wave == 4 || wave == 5, 1)) {
        Loader L;
        L.lw = wave - 4;
        L.rx = __builtin_amdgcn_make_buffer_rsrc((void*)a.x, 0, NB * SQ * DM * 2, 0x00020000);
        L.rwq = __builtin_amdgcn_make_buffer_rsrc((void*)a.wq, 0, DM * HD * 2, 0x00020000);
        const unsigned r8 = lane >> 3, c8 = (lane & 7) ^ (lane >> 4);
        L.vX[0] = r8 * (DM * 2) + c8 * 16;
        L.vX[1] = r8 * (DM * 2) + (c8 ^ 4) * 16;
#pragma unroll
        for (int e = 0; e < 3; ++e) {
            const unsigned p = e * 64 + lane, row = (p * 2731) >> 16, cc = p - row * 24;
            L.vW[e] = row * (HD * 2) + (cc ^ (((row >> 1) & 1) << 2)) * 16;
        }
        L.r1 = ((((j & 3) * 5) >> 1) + ((i * 5) >> 1)) % 10;
        L.r2 = rot_heads(j, i);
        L.sX = (64 * j + L.lw * 32) * (DM * 2);
        L.sW = L.lw * 32 * (HD * 2) + head * (DH * 2);
        issue_p1(L, 0, 0);
        issue_p1(L, 1, SLOT);
        L.rk = __builtin_amdgcn_make_buffer_rsrc((void*)a.kc, 0, NB * NH * SK * DH * 2, 0x00020000);
        L.rv = __builtin_amdgcn_make_buffer_rsrc((void*)a.vc, 0, NB * NH * SK * DH * 2, 0x00020000);
        L.rwo = __builtin_amdgcn_make_buffer_rsrc((void*)a.wo, 0, HD * DM * 2, 0x00020000);
        L.vKV[0] = r8 * (DH * 2) + c8 * 16;
        L.vKV[1] = r8 * (DH * 2) + (c8 ^ 4) * 16;
#pragma unroll
        for (int e = 0; e < 5; ++e) {
            const unsigned p = e * 64 + lane, row = (p * 3277) >> 16, cc = p - row * 20;
            L.vO[e] = row * (DM * 2) + (cc ^ (((row >> 3) & 1) << 1)) * 16;
        }
        L.sK = ((2 * j + L.lw) * NH + head) * (SK * DH * 2);
        L.sV = ((2 * j + L.lw) * NH + xg * 4) * (SK * DH * 2);
        L.sO = (xg * 4 * DH + L.lw * 32) * (DM * 2) + i * 320;
        const unsigned pK = PK0 + loc % PMOD, pV = PV0 + loc % PMOD;
        {
            unsigned base = 2 * SLOT;
#pragma unroll 1
            for (unsigned t = 0; t < 8; ++t) {
                if (t - 1 == pK || t - 1 == pV) wait_vm_barrier<28>(); else wait_vm_barrier<16>();
                if (t == pK) issue_tile(L, L.rk, KREG, L.sK);
                if (t == pV) issue_tile(L, L.rv, V0REG, L.sV + L.r2 * (SK * DH * 2));
                issue_p1(L, t + 2, base);
                base = base == 2 * SLOT ? 0 : base + SLOT;
            }
        }
        if (7 == pK || 7 == pV) wait_vm_barrier<28>(); else wait_vm_barrier<16>();
        issue_p2<false>(L, (10 % 3) * SLOT, 0, 0);
        wait_vm_barrier<10>(); issue_p2<false>(L, (11 % 3) * SLOT, 0, 1);
        wait_vm_barrier<10>(); issue_p2<false>(L, (12 % 3) * SLOT, 0, 2);
        wait_vm_barrier<10>(); issue_p2<true>(L, (13 % 3) * SLOT, 1, 0);
        {
            unsigned hh = 1, dc = 1, base = (14 % 3) * SLOT;
#pragma unroll 1
            for (unsigned t = 12; t < 20; ++t) {
                wait_vm_barrier<14>(); issue_p2<true>(L, base, hh, dc);
                if (++dc == 3) { dc = 0; ++hh; }
                base = base == 2 * SLOT ? 0 : base + SLOT;
            }
        }
        wait_vm_barrier<14>();
        wait_vm_barrier<0>();
    } else if (wave >= 6) {
        const int b = wave - 6;
        u64 tag;
        asm volatile("global_load_dwordx2 %0, %1, off sc1" : "=v"(tag) : "v"((const gu64*)(a.ws + WS_EPOCH + j * 256)) : "memory");
        BARRIER(); BARRIER(); BARRIER(); BARRIER();
        asm volatile("s_waitcnt vmcnt(0)" : "+v"(tag)::"memory");
        tag += 1;
        if (wave == 7 && lane == 0) *(LAS u64*)(uintptr_t)TAGW = tag;
        BARRIER_LGKM();
#pragma unroll 1
        for (int t = 5; t < 12; ++t) BARRIER();
        __builtin_amdgcn_s_setprio(3);
        const int h = lane >> 5, r = lane & 31;
        float S[16];
        {
            f32x4 u[4], v[4];
#pragma unroll
            for (int gq = 0; gq < 4; ++gq) {
                u[gq] = *(LAS f32x4*)(uintptr_t)(SREG + (2 * b) * 4096 + gq * 1024 + lane * 16);
                v[gq] = *(LAS f32x4*)(uintptr_t)(SREG + (2 * b + 1) * 4096 + gq * 1024 + lane * 16);
            }
            SCHED0();
#pragma unroll
            for (int gq = 0; gq < 4; ++gq)
#pragma unroll
                for (int e = 0; e < 4; ++e) S[4 * gq + e] = u[gq][e] + v[gq][e];
        }
        float m = S[0];
#pragma unroll
        for (int e = 1; e < 16; ++e) m = fmaxf(m, S[e]);
        m = half_max(m);
        const float c = 0.07216878364870322f * 1.4426950408889634f;
        const float mc = m * c;
#pragma unroll
        for (int e = 0; e < 16; ++e) S[e] = __builtin_fmaf(S[e], c, -mc);
#pragma unroll
        for (int e = 0; e < 16; e += 4) asm volatile("" : "+v"(S[e]), "+v"(S[e + 1]), "+v"(S[e + 2]), "+v"(S[e + 3]));
        BARRIER();
        float sum = 0.f;
#pragma unroll
        for (int e = 0; e < 16; ++e) { S[e] = __builtin_amdgcn_exp2f(S[e]); sum += S[e]; }
        float inv = __builtin_amdgcn_rcpf(half_sum(sum));
#pragma unroll
        for (int e = 0; e < 16; e += 4) asm volatile("" : "+v"(S[e]), "+v"(S[e + 1]), "+v"(S[e + 2]), "+v"(S[e + 3]));
        asm volatile("" : "+v"(inv));
        BARRIER();
        {
            u32x4 f0, f1;
#pragma unroll
            for (int e = 0; e < 2; ++e) {
                f0[e] = pk2(S[2 * e] * inv, S[2 * e + 1] * inv);
                f0[2 + e] = pk2(S[8 + 2 * e] * inv, S[8 + 2 * e + 1] * inv);
                f1[e] = pk2(S[4 + 2 * e] * inv, S[4 + 2 * e + 1] * inv);
                f1[2 + e] = pk2(S[12 + 2 * e] * inv, S[12 + 2 * e + 1] * inv);
            }
            const unsigned pb = WS_P + ((j * NH + head) * 2 + b) * 2048 + (r >> 4) * 1024 + (r & 15) * 16;
            __builtin_amdgcn_raw_buffer_store_b128(f0, rws, pb + h * 256, 0, 16);
            __builtin_amdgcn_raw_buffer_store_b128(f1, rws, pb + (2 + h) * 256, 0, 16);
        }
        __builtin_amdgcn_s_setprio(0);
        BARRIER();
        BARRIER_VM0();
        if (wave == 7 && lane < 4) __hip_atomic_store((gu64*)(a.ws + WS_F1 + ((j * 4 + lane) * 8 + xg) * 128) + i, tag, RLX_AGENT);
#pragma unroll 1
        for (int t = 16; t <= POLL_T; ++t) BARRIER();
        {
            const gu64* f1 = (const gu64*)(a.ws + WS_F1 + blockIdx.x * 128) + (lane & 3);
            u64 seen;
            if (wave == 7) {
                asm volatile("global_load_dwordx2 %0, %1, off sc1" : "=v"(seen) : "v"(f1) : "memory");
#pragma unroll 1
                for (int t = POLL_T + 1; t < PLOAD_T; ++t) BARRIER();
                asm volatile("s_waitcnt vmcnt(0)" : "+v"(seen)::"memory");
                wait_tags(f1, tag, seen);
            } else {
#pragma unroll 1
                for (int t = POLL_T + 1; t < PLOAD_T; ++t) BARRIER();
            }
        }
#pragma unroll 1
        for (int t = PLOAD_T; t < T1; ++t) BARRIER();
    } else {
        const int wb = wave >> 1, wd = wave & 1;
        const int g = lane >> 4, c = lane & 15;
        unsigned aV[2], aO[2];
        {
            const int row = 32 * wb + c;
            aV[0] = row * 128 + ((g ^ ((c >> 1) & 7)) * 16);
            aV[1] = aV[0] ^ 64;
            const int q = (lane >> 2) & 3, p = lane & 3, m = g & 1;
            const unsigned com = 8192 + (8 * g + q) * 320 + p * 8;
            aO[0] = com + ((5 * wd) ^ m) * 32;
            aO[1] = com + ((5 * wd + 1) ^ m) * 32;
        }
        f32x4 acc2[2][5];
        u32x4 vw[4][5];
        s16x8 C0[2], D0[5], C1[2], D1[5];
        u32x4 pf[4][2];
        unsigned bV[2], bO[2];
        auto rd2 = [&](s16x8 (&A)[2], s16x8 (&Bf)[5], int ks) {
#pragma unroll
            for (int kt = 0; kt < 2; ++kt) A[kt] = lds_b128(bV[ks] + kt * 2048);
#pragma unroll
            for (int nt = 0; nt < 5; ++nt) {
                const unsigned ad = bO[nt & 1] + (nt & ~1) * 32 + ks * 32 * 320;
                Bf[nt] = cat(lds_tr(ad), lds_tr(ad + 4 * 320));
            }
        };
        auto mm2 = [&](s16x8 (&A)[2], s16x8 (&Bf)[5]) {
#pragma unroll
            for (int nt = 0; nt < 5; ++nt)
#pragma unroll
                for (int kt = 0; kt < 2; ++kt) acc2[kt][nt] = MFMA16(A[kt], Bf[nt], acc2[kt][nt]);
        };
        auto mm20 = [&](s16x8 (&A)[2], s16x8 (&Bf)[5]) {
#pragma unroll
            for (int nt = 0; nt < 5; ++nt)
#pragma unroll
                for (int kt = 0; kt < 2; ++kt) acc2[kt][nt] = MFMA16(A[kt], Bf[nt], ((f32x4){0.f, 0.f, 0.f, 0.f}));
        };
        auto pack = [&](u32x4 (&o)[5]) {
#pragma unroll
            for (int nt = 0; nt < 5; ++nt) {
                o[nt][0] = pk2(acc2[0][nt][0], acc2[0][nt][1]); o[nt][1] = pk2(acc2[0][nt][2], acc2[0][nt][3]);
                o[nt][2] = pk2(acc2[1][nt][0], acc2[1][nt][1]); o[nt][3] = pk2(acc2[1][nt][2], acc2[1][nt][3]);
                asm volatile("" : "+v"(o[nt]));
            }
        };
        auto bases = [&](int u) {
            const unsigned so = ((10 + u) % NSLOT) * SLOT, sv = u < 3 ? V0REG + u * 8192 : so;
            bV[0] = aV[0] + sv; bV[1] = aV[1] + sv; bO[0] = aO[0] + so; bO[1] = aO[1] + so;
            asm volatile("" : "+v"(bV[0]), "+v"(bV[1]), "+v"(bO[0]), "+v"(bO[1]));
        };
        {
            const int h = lane >> 5, r = lane & 31;
            unsigned aW[3];
            {
                const int grp = (lane >> 4) & 1, q = (lane >> 2) & 3, p = lane & 3;
                const int bs = ((p & 1) << 1) | (p >> 1);
                const int m = (q >> 1) & 1;
#pragma unroll
                for (int t = 0; t < 3; ++t)
                    aW[t] = 8192 + (8 * h + q) * 384 + grp * 32 + bs * 8 + (((3 * wd + t) ^ m) * 64);
            }
            unsigned aX[4];
            {
                const int row = 32 * wb + r;
#pragma unroll
                for (int ks = 0; ks < 4; ++ks) aX[ks] = (row * 128 + ((h ^ ((row >> 1) & 7)) * 16)) ^ (32 * ks);
            }
            f32x16 acc[3];
#pragma unroll
            for (int t = 0; t < 3; ++t)
#pragma unroll
                for (int e = 0; e < 16; ++e) acc[t][e] = 0.f;
            s16x8 A0[2][3], B0[2], A1[2][3], B1[2];
            auto rd = [&](s16x8 (&A)[2][3], s16x8 (&Bf)[2], unsigned so, int ds) {
#pragma unroll
                for (int s = 0; s < 2; ++s) {
                    const int ks = 2 * ds + s;
                    Bf[s] = lds_b128(aX[ks] + so);
#pragma unroll
                    for (int t = 0; t < 3; ++t)
                        A[s][t] = cat(lds_tr(aW[t] + so + ks * 16 * 384), lds_tr(aW[t] + so + (ks * 16 + 4) * 384));
                }
            };
            auto mm = [&](s16x8 (&A)[2][3], s16x8 (&Bf)[2]) {
#pragma unroll
                for (int s = 0; s < 2; ++s)
#pragma unroll
                    for (int t = 0; t < 3; ++t) acc[t] = MFMA32(A[s][t], Bf[s], acc[t]);
            };
            BARRIER_LGKM();
            SCHED0();
            rd(A0, B0, 0, 0);
            SCHED0();
            rd(A1, B1, 0, 1);
            mm(A0, B0);
            SGB_P1();
            SCHED0();
            unsigned so = SLOT;
#pragma unroll 1
            for (int t = 1; t < 10; ++t) {
                BARRIER_LGKM();
                SCHED0();
                rd(A0, B0, so, 0);
                mm(A1, B1);
                SGB_P1();
                SCHED0();
                rd(A1, B1, so, 1);
                mm(A0, B0);
                SGB_P1();
                SCHED0();
                so = so == 2 * SLOT ? 0 : so + SLOT;
            }
            s16x8 Kf[3][2];
            {
                const int row = 32 * wb + r;
                const unsigned kb = KREG + row * 128;
                const int m = (row >> 1) & 7;
#pragma unroll
                for (int t = 0; t < 3; ++t)
#pragma unroll
                    for (int s = 0; s < 2; ++s) {
                        const int cc = 12 * wd + 4 * t + 2 * s + h;
                        Kf[t][s] = lds_b128(kb + (cc >> 3) * 8192 + ((cc & 7) ^ m) * 16);
                    }
            }
            BARRIER_LGKM();
            bases(0);
            SCHED0();
            rd2(C0, D0, 0);
            mm(A1, B1);
            SGB(0x8, 1); SGB(0x100, 4); SGB(0x8, 1); SGB(0x100, 4); SGB(0x8, 1); SGB(0x100, 4); SGB(0x8, 3);
            SCHED0();
            f32x16 S;
#pragma unroll
            for (int e = 0; e < 16; ++e) S[e] = 0.f;
#pragma unroll
            for (int t = 0; t < 3; ++t)
#pragma unroll
                for (int s = 0; s < 2; ++s) {
                    u32x4 qp;
#pragma unroll
                    for (int e = 0; e < 4; ++e) qp[e] = pk2(acc[t][8 * s + 2 * e], acc[t][8 * s + 2 * e + 1]);
                    S = MFMA32(Kf[t][s], __builtin_bit_cast(s16x8, qp), S);
                }
            SCHED0();
            rd2(C1, D1, 1);
            mm20(C0, D0);
            SGB_P2();
            SCHED0();
            const unsigned sx = SREG + wave * 4096 + lane * 16;
#pragma unroll
            for (int gq = 0; gq < 4; ++gq) {
                const f32x4 v = {S[4 * gq], S[4 * gq + 1], S[4 * gq + 2], S[4 * gq + 3]};
                *(LAS f32x4*)(uintptr_t)(sx + gq * 1024) = v;
            }
        }
        {
#pragma unroll
            for (int u = 1; u < 12; ++u) {
                const int hh = u / 3, dc = u % 3, t = 10 + u;
                BARRIER_LGKM();
                if (t == PLOAD_T) {
                    const int r2 = rot_heads(j, i);
#pragma unroll
                    for (int h2 = 0; h2 < 4; ++h2) {
                        const unsigned pb = WS_P + ((j * NH + xg * 4 + ((h2 + r2) & 3)) * 2 + wb) * 2048 + lane * 16;
                        pf[h2][0] = __builtin_amdgcn_raw_buffer_load_b128(rws, pb, 0, 16);
                        pf[h2][1] = __builtin_amdgcn_raw_buffer_load_b128(rws, pb + 1024, 0, 16);
                    }
                }
                bases(u);
                SCHED0();
                rd2(C0, D0, 0);
                mm2(C1, D1);
                SGB_P2();
                SCHED0();
                if (dc == 0) { pack(vw[hh - 1]); SCHED0(); }
                rd2(C1, D1, 1);
                if (dc == 0) mm20(C0, D0); else mm2(C0, D0);
                SGB_P2();
                SCHED0();
            }
            mm2(C1, D1);
            pack(vw[3]);
            f32x4 o[5][2];
#pragma unroll
            for (int nt = 0; nt < 5; ++nt) { o[nt][0] = (f32x4){0.f, 0.f, 0.f, 0.f}; o[nt][1] = o[nt][0]; }
#pragma unroll
            for (int hh = 0; hh < 4; ++hh) {
#pragma unroll
                for (int nt = 0; nt < 5; ++nt) {
                    o[nt][0] = MFMA16(__builtin_bit_cast(s16x8, vw[hh][nt]), __builtin_bit_cast(s16x8, pf[hh][0]), o[nt][0]);
                    o[nt][1] = MFMA16(__builtin_bit_cast(s16x8, vw[hh][nt]), __builtin_bit_cast(s16x8, pf[hh][1]), o[nt][1]);
                }
            }
            const unsigned pp = WS_PART + (((j * 4 + i) * 8 + xg) * 8 + wb * 4 + (c >> 3)) * PART_CH + wd * 1280;
            const unsigned l32 = g * 8 + (c & 7);
#pragma unroll
            for (int qt = 0; qt < 2; ++qt) {
#pragma unroll
                for (int it = 0; it < 2; ++it) {
                    const u32x4 w = {pk2(o[2 * it][qt][0], o[2 * it][qt][1]), pk2(o[2 * it][qt][2], o[2 * it][qt][3]),
                                     pk2(o[2 * it + 1][qt][0], o[2 * it + 1][qt][1]), pk2(o[2 * it + 1][qt][2], o[2 * it + 1][qt][3])};
                    __builtin_amdgcn_raw_buffer_store_b128(w, rws, pp + qt * 2 * PART_CH + it * 512 + l32 * 16, 0, 16);
                }
                const u32x2 w2 = {pk2(o[4][qt][0], o[4][qt][1]), pk2(o[4][qt][2], o[4][qt][3])};
                __builtin_amdgcn_raw_buffer_store_b64(w2, rws, pp + qt * 2 * PART_CH + 1024 + l32 * 8, 0, 16);
            }
        }
    }
    BARRIER_VM0();
    if (wave >= 4 && wave < 7) {
        const u64 tag = *(LAS u64*)(uintptr_t)TAGW;
        if (wave == 6 && lane < 8) __hip_atomic_store((gu64*)(a.ws + WS_F2 + ((j * 4 + i) * 8 + lane) * 128) + xg, tag, RLX_AGENT);
        const int cw = wave - 4, hi = lane >> 5, l32 = lane & 31, cl = l32 & 7, g = l32 >> 3;
        unsigned pv = cw < 2 ? cw * 1280 + hi * 512 + l32 * 16 : hi * 1280 + 1024 + l32 * 8;
        const unsigned ps = WS_PART + (((j * 4 + i) * 8) * 8 + xg) * PART_CH;
        unsigned ov = (64 * j + 8 * xg + cl) * (DM * 2) + i * 320 + (cw < 2 ? 80 * cw + 32 * hi + 4 * g : 80 * hi + 64 + 4 * g) * 2;
        const __amdgpu_buffer_rsrc_t ro = __builtin_amdgcn_make_buffer_rsrc(a.out, 0, NB * SQ * DM * 2, 0x00020000);
        asm volatile("" : "+v"(pv), "+v"(ov));
        const gu64* f2 = (const gu64*)(a.ws + WS_F2 + blockIdx.x * 128) + (lane & 7);
        u64 p0, p1, p2, p3;
#define POLL(x) asm volatile("global_load_dwordx2 %0, %1, off sc1" : "=v"(x) : "v"(f2) : "memory")
#define POLL_WAIT(x) asm volatile("s_waitcnt vmcnt(3)" : "+v"(x)::"memory")
        POLL(p0); __builtin_amdgcn_s_sleep(2); POLL(p1); __builtin_amdgcn_s_sleep(2); POLL(p2); __builtin_amdgcn_s_sleep(2); POLL(p3);
        for (unsigned spins = 0; spins < kSpinCap; ++spins) {
            POLL_WAIT(p0); if (__all(p0 == tag)) break; POLL(p0);
            POLL_WAIT(p1); if (__all(p1 == tag)) break; POLL(p1);
            POLL_WAIT(p2); if (__all(p2 == tag)) break; POLL(p2);
            POLL_WAIT(p3); if (__all(p3 == tag)) break; POLL(p3);
        }
        if (cw < 2) {
            u32x4 v[8];
#pragma unroll
            for (int s = 0; s < 8; ++s) v[s] = __builtin_amdgcn_raw_buffer_load_b128(rws, pv, ps + s * 8 * PART_CH, 16);
            SCHED0();
            float t[8];
#pragma unroll
            for (int e = 0; e < 4; ++e) {
                t[2 * e] = ((bflo(v[0][e]) + bflo(v[1][e])) + (bflo(v[2][e]) + bflo(v[3][e]))) + ((bflo(v[4][e]) + bflo(v[5][e])) + (bflo(v[6][e]) + bflo(v[7][e])));
                t[2 * e + 1] = ((bfhi(v[0][e]) + bfhi(v[1][e])) + (bfhi(v[2][e]) + bfhi(v[3][e]))) + ((bfhi(v[4][e]) + bfhi(v[5][e])) + (bfhi(v[6][e]) + bfhi(v[7][e])));
            }
            const u32x2 o0 = {pk2(t[0], t[1]), pk2(t[2], t[3])}, o1 = {pk2(t[4], t[5]), pk2(t[6], t[7])};
            asm volatile("" ::"v"(p0), "v"(p1), "v"(p2), "v"(p3), "v"(o0));
            __builtin_amdgcn_raw_buffer_store_b64(o0, ro, ov, 0, 0);
            __builtin_amdgcn_raw_buffer_store_b64(o1, ro, ov + 32, 0, 0);
        } else {
            u32x2 v[8];
#pragma unroll
            for (int s = 0; s < 8; ++s) v[s] = __builtin_amdgcn_raw_buffer_load_b64(rws, pv, ps + s * 8 * PART_CH, 16);
            SCHED0();
            float t[4];
#pragma unroll
            for (int e = 0; e < 2; ++e) {
                t[2 * e] = ((bflo(v[0][e]) + bflo(v[1][e])) + (bflo(v[2][e]) + bflo(v[3][e]))) + ((bflo(v[4][e]) + bflo(v[5][e])) + (bflo(v[6][e]) + bflo(v[7][e])));
                t[2 * e + 1] = ((bfhi(v[0][e]) + bfhi(v[1][e])) + (bfhi(v[2][e]) + bfhi(v[3][e]))) + ((bfhi(v[4][e]) + bfhi(v[5][e])) + (bfhi(v[6][e]) + bfhi(v[7][e])));
            }
            const u32x2 o0 = {pk2(t[0], t[1]), pk2(t[2], t[3])};
            asm volatile("" ::"v"(p0), "v"(p1), "v"(p2), "v"(p3), "v"(o0));
            __builtin_amdgcn_raw_buffer_store_b64(o0, ro, ov, 0, 0);
            if (xg == 0 && i == 0 && lane == 0) __hip_atomic_store((gu64*)(a.ws + WS_EPOCH + j * 256), tag, RLX_AGENT);
        }
    }
}

extern "C" void kernel_launch(void* const* d_in, const int* in_sizes, int n_in, void* d_out, int out_size,
                              void* d_ws, size_t ws_size, hipStream_t stream) {
    Args a;
    a.x = d_in[0]; a.kc = d_in[1]; a.vc = d_in[2]; a.wq = d_in[3]; a.wo = d_in[4];
    a.out = d_out; a.ws = (char*)d_ws;
    gqa_fused<<<256, 512, 0, stream>>>(a);
}
```
